```python
import torch

def _softmax(x, axis=-1):
    return torch.softmax(x, dim=axis)
def _gelu(x):
    return torch.nn.functional.gelu(x, approximate="tanh")
def _silu(x):
    return torch.nn.functional.silu(x)
def _sigmoid(x):
    return torch.sigmoid(x)
def _layernorm(x, axis=-1, eps=1e-5):
    m = x.mean(dim=axis, keepdim=True)
    v = x.var(dim=axis, keepdim=True, unbiased=False)
    return (x - m) / torch.sqrt(v + eps)
def _scatter_add(tgt, idx, src, axis=0):
    return tgt.clone().index_add_(axis, idx, src)
def _segment_sum(src, seg, n):
    out = torch.zeros((n,) + src.shape[1:], dtype=src.dtype)
    return out.index_add_(0, seg, src)
def _pad(x, pw):
    flat = tuple(v for pair in reversed(pw) for v in pair)
    return torch.nn.functional.pad(x, flat)

kernel_name = 'concat_heads__gqa_block_cached__mha_block_cached__near_ridge'

def setup_inputs():

    _w = (lambda a: (lambda t: t.to(torch.bfloat16) if t.is_floating_point() else t)(torch.as_tensor(a)))
    def _A_setup():
        B, Sq, D = 32, 48, 512
        Hq, Hkv, Sk, Dh = 32, 8, 512, 128
        assert Hq % Hkv == 0, (Hq, Hkv)
        return {
            "x": _w((lambda *s: torch.randn(*s, dtype=torch.bfloat16) * 0.8)(B, Sq, D)),
            "k_cache": _w((lambda *s: torch.randn(*s, dtype=torch.bfloat16) * 0.8)(B, Hkv, Sk, Dh)),
            "v_cache": _w((lambda *s: torch.randn(*s, dtype=torch.bfloat16) * 0.05)(B, Hkv, Sk, Dh)),
            "wq": _w((lambda *s: torch.randn(*s, dtype=torch.bfloat16) * 0.8)(D, Hq * Dh) * (D ** -0.5)),
            "wo": _w((lambda *s: torch.randn(*s, dtype=torch.bfloat16) * 0.05)(Hq * Dh, D) * ((Hq * Dh) ** -0.5)),
        }
    _A_in = _A_setup()

    def _B_setup():
        B, Sq, D = 32, 48, 512
        H, Sk, Dh = 32, 512, 128
        return {
            "x": _w((lambda *s: torch.randn(*s, dtype=torch.bfloat16) * 0.8)(B, Sq, D)),
            "k_cache": _w((lambda *s: torch.randn(*s, dtype=torch.bfloat16) * 0.8)(B, H, Sk, Dh)),
            "v_cache": _w((lambda *s: torch.randn(*s, dtype=torch.bfloat16) * 0.05)(B, H, Sk, Dh)),
            "wq": _w((lambda *s: torch.randn(*s, dtype=torch.bfloat16) * 0.8)(D, H * Dh) * (D ** -0.5)),
            "wo": _w((lambda *s: torch.randn(*s, dtype=torch.bfloat16) * 0.05)(H * Dh, D) * ((H * Dh) ** -0.5)),
        }
    _B_in = _B_setup()

    return {
        "A_x": _A_in["x"],
        "A_k_cache": _A_in["k_cache"],
        "A_v_cache": _A_in["v_cache"],
        "A_wq": _A_in["wq"],
        "A_wo": _A_in["wo"],
        "B_x": _B_in["x"],
        "B_k_cache": _B_in["k_cache"],
        "B_v_cache": _B_in["v_cache"],
        "B_wq": _B_in["wq"],
        "B_wo": _B_in["wo"],
    }

def reference(A_x, A_k_cache, A_v_cache, A_wq, A_wo, B_x, B_k_cache, B_v_cache, B_wq, B_wo):
    def _A_ref(x, k_cache, v_cache, wq, wo):
        B, Sq, D = x.shape
        Hq, Dh = 32, 128
        Hkv = k_cache.shape[1]
        rep = Hq // Hkv
        q = (lambda x, *d: x.permute(*d))((x @ wq).reshape(B, Sq, Hq, Dh), 0, 2, 1, 3)
        k = (lambda x, n, axis: torch.repeat_interleave(x, n, dim=axis))(k_cache, rep, axis=1)
        v = (lambda x, n, axis: torch.repeat_interleave(x, n, dim=axis))(v_cache, rep, axis=1)
        scores = torch.einsum("bhqd,bhkd->bhqk", q, k) * (Dh ** -0.5)
        p = _softmax(scores, axis=-1)
        o = torch.einsum("bhqk,bhkd->bhqd", p, v)
        return (lambda x, *d: x.permute(*d))(o, 0, 2, 1, 3).reshape(B, Sq, Hq * Dh) @ wo

    def _B_ref(x, k_cache, v_cache, wq, wo):
        B, Sq, D = x.shape
        H, Dh = 32, 128
        q = (lambda x, *d: x.permute(*d))((x @ wq).reshape(B, Sq, H, Dh), 0, 2, 1, 3)
        scores = torch.einsum("bhqd,bhkd->bhqk", q, k_cache) * (Dh ** -0.5)
        p = _softmax(scores, axis=-1)
        o = torch.einsum("bhqk,bhkd->bhqd", p, v_cache)
        return (lambda x, *d: x.permute(*d))(o, 0, 2, 1, 3).reshape(B, Sq, H * Dh) @ wo

    a = _A_ref(A_x, A_k_cache, A_v_cache, A_wq, A_wo)
    b = _B_ref(B_x, B_k_cache, B_v_cache, B_wq, B_wo)
    return torch.concatenate([a, b], axis=-1)
```

```cpp
#include <hip/hip_runtime.h>
#include <hip/hip_bf16.h>
typedef unsigned short u16;
__device__ __forceinline__ float bf2f(u16 v){ return __uint_as_float(((unsigned)v)<<16); }
__device__ __forceinline__ u16 f2bf(float f){ __hip_bfloat16 h = __float2bfloat16(f); return *(u16*)&h; }
__global__ void gemm_naive(const u16* A, const u16* B, u16* C, int M, int N, int K, int ldc){
  __shared__ float As[16][17], Bs[16][17];
  int tx=threadIdx.x&15, ty=threadIdx.x>>4;
  int r=blockIdx.y*16+ty, c=blockIdx.x*16+tx;
  float acc=0;
  for(int k0=0;k0<K;k0+=16){
    As[ty][tx]=bf2f(A[(size_t)r*K+k0+tx]);
    Bs[ty][tx]=bf2f(B[(size_t)(k0+ty)*N+c]);
    __syncthreads();
    for(int k=0;k<16;++k) acc+=As[ty][k]*Bs[k][tx];
    __syncthreads();
  }
  C[(size_t)r*ldc+c]=f2bf(acc);
}
__global__ void attn_naive(const u16* Q, const u16* K, const u16* V, u16* O, int Hkv){
  int q=blockIdx.x, h=blockIdx.y, b=blockIdx.z; int t=threadIdx.x;
  int kvh=h/(32/Hkv);
  __shared__ float qs[128], s[512], red[128];
  qs[t]=bf2f(Q[((size_t)b*48+q)*4096+h*128+t]);
  __syncthreads();
  const u16* Kh=K+((size_t)b*Hkv+kvh)*512*128; const u16* Vh=V+((size_t)b*Hkv+kvh)*512*128;
  float mx=-1e30f;
  for(int j=t;j<512;j+=128){ float a=0; for(int d=0;d<128;++d) a+=qs[d]*bf2f(Kh[j*128+d]); a*=0.08838834764831845f; s[j]=a; mx=fmaxf(mx,a);}
  red[t]=mx; __syncthreads();
  for(int o=64;o>0;o>>=1){ if(t<o) red[t]=fmaxf(red[t],red[t+o]); __syncthreads(); }
  mx=red[0]; __syncthreads();
  float sum=0; for(int j=t;j<512;j+=128){ float e=__expf(s[j]-mx); s[j]=e; sum+=e; }
  red[t]=sum; __syncthreads();
  for(int o=64;o>0;o>>=1){ if(t<o) red[t]+=red[t+o]; __syncthreads(); }
  sum=red[0];
  float a=0; for(int j=0;j<512;++j) a+=s[j]*bf2f(Vh[j*128+t]);
  O[((size_t)b*48+q)*4096+h*128+t]=f2bf(a/sum);
}
extern "C" void kernel_launch(void* const* d_in, const int* in_sizes, int n_in, void* d_out, int out_size, void* d_ws, size_t ws_size, hipStream_t stream) {
  u16* ws=(u16*)d_ws; size_t nq=(size_t)1536*4096;
  for(int br=0;br<2;++br){
    const u16* x=(const u16*)d_in[br*5+0]; const u16* k=(const u16*)d_in[br*5+1]; const u16* v=(const u16*)d_in[br*5+2];
    const u16* wq=(const u16*)d_in[br*5+3]; const u16* wo=(const u16*)d_in[br*5+4];
    u16* Q=ws+(size_t)br*2*nq; u16* O=Q+nq;
    gemm_naive<<<dim3(4096/16,1536/16),256,0,stream>>>(x,wq,Q,1536,4096,512,4096);
    attn_naive<<<dim3(48,32,32),128,0,stream>>>(Q,k,v,O,br==0?8:32);
    gemm_naive<<<dim3(512/16,1536/16),256,0,stream>>>(O,wo,(u16*)d_out+br*512,1536,512,4096,1024);
  }
}
```

```cpp
#include <hip/hip_runtime.h>
#include <stdint.h>
#include <utility>

typedef __bf16 bf16x8 __attribute__((ext_vector_type(8)));
typedef __bf16 bf16x4 __attribute__((ext_vector_type(4)));
typedef __bf16 bf16x2 __attribute__((ext_vector_type(2)));
typedef float f32x4 __attribute__((ext_vector_type(4)));
typedef float f32x2 __attribute__((ext_vector_type(2)));
typedef unsigned u32x4 __attribute__((ext_vector_type(4)));
typedef unsigned long long u64;
#define LDS_AS __attribute__((address_space(3)))
#define DI __device__ __forceinline__
#define SBAR() __builtin_amdgcn_sched_barrier(0)

#define STAMPS 0
#define GATE0 4
#define GATEK 24
#define PAUSE 1
#define APRIO 1
#define NSA 4

constexpr unsigned RB = 0;
constexpr unsigned RX = 65536;
constexpr unsigned RA = 114688;
constexpr unsigned RS = 163584, LDS_BYTES = 163840;
constexpr unsigned W_ABAR = RS + 0, W_BPROG = RS + 16, W_BQP = RS + 32, W_PAUSE = RS + 48, W_OB = RS + 64  , W_XB = RS + 96  ;
constexpr float QSCALE = 0.08838834764831845f * 1.4426950408889634f;
constexpr float THR = 8.0f;

struct Params {
  const char* x[2]; const char* k[2]; const char* v[2]; const char* wq[2]; const char* wo[2];
  char* part; u64* stamps;
};

DI u32x4 make_srd(const char* p, unsigned bytes) { u32x4 s = {(unsigned)(uintptr_t)p, (unsigned)((uintptr_t)p >> 32), bytes, 0x00020000u}; return s; }
template <int N> DI void wait_vm() { asm volatile("s_waitcnt vmcnt(%0)" :: "i"(N) : "memory"); }
DI void wait_lgkm0() { asm volatile("s_waitcnt lgkmcnt(0)" ::: "memory"); }
DI void set_m0(unsigned dst) { asm volatile("s_mov_b32 m0, %0\n\ts_nop 0" :: "s"(dst) : "memory"); }
template <int OFF, int NT = 1> DI void dma(u32x4 srd, unsigned v, unsigned so) {
  if constexpr (NT) asm volatile("buffer_load_dwordx4 %0, %1, %3 offen offset:%2 nt lds" :: "v"(v), "s"(srd), "i"(OFF), "s"(so) : "memory");
  else asm volatile("buffer_load_dwordx4 %0, %1, %3 offen offset:%2 lds" :: "v"(v), "s"(srd), "i"(OFF), "s"(so) : "memory");
}
template <int NT = 1> DI void dma4(unsigned dst, u32x4 srd, unsigned v0, unsigned v1, unsigned v2, unsigned v3, unsigned so) {
  set_m0(dst); dma<0, NT>(srd, v0, so); dma<1024, NT>(srd, v1, so); dma<2048, NT>(srd, v2, so); dma<3072, NT>(srd, v3, so);
}
DI bf16x4 tr_read(unsigned a) { return __builtin_amdgcn_ds_read_tr16_b64_v4bf16((LDS_AS bf16x4*)a); }
DI bf16x8 ld128(unsigned a) { return *(LDS_AS bf16x8*)a; }
DI bf16x8 cat(bf16x4 a, bf16x4 b) { return __builtin_shufflevector(a, b, 0, 1, 2, 3, 4, 5, 6, 7); }
DI f32x4 mfma(bf16x8 a, bf16x8 b, f32x4 c) { return __builtin_amdgcn_mfma_f32_16x16x32_bf16(a, b, c, 0, 0, 0); }
DI unsigned cvtpk(float lo, float hi) { f32x2 v = {lo, hi}; bf16x2 b = __builtin_convertvector(v, bf16x2); return __builtin_bit_cast(unsigned, b); }
DI u32x4 pack_pair(f32x4 t0, f32x4 t1, float s, bool odd) {
  unsigned a0 = cvtpk(t0[0] * s, t0[1] * s), a1 = cvtpk(t0[2] * s, t0[3] * s), b0 = cvtpk(t1[0] * s, t1[1] * s), b1 = cvtpk(t1[2] * s, t1[3] * s);
  u32x4 r = {odd ? b0 : a0, odd ? b1 : a1, odd ? a0 : b0, odd ? a1 : b1}; return r;
}
DI unsigned fresh_lane() { unsigned l; asm volatile("v_mbcnt_lo_u32_b32 %0, -1, 0\n\tv_mbcnt_hi_u32_b32 %0, -1, %0" : "=v"(l)); return l; }
DI float xmax16_32(float d) {
  auto r = __builtin_amdgcn_permlane16_swap(__float_as_uint(d), __float_as_uint(d), false, false);
  d = fmaxf(__uint_as_float(r[0]), __uint_as_float(r[1]));
  auto u = __builtin_amdgcn_permlane32_swap(__float_as_uint(d), __float_as_uint(d), false, false);
  return fmaxf(__uint_as_float(u[0]), __uint_as_float(u[1]));
}
DI void lds_add(unsigned addr, unsigned val) {
  asm volatile("s_mov_b64 exec, 1\n\tds_add_u32 %0, %1\n\ts_mov_b64 exec, -1" :: "v"(addr), "v"(val) : "memory");
}
DI void lds_wait_ge(unsigned addr, unsigned target) {
  unsigned v, sv, cnt = 1u << 18;
  asm volatile("1:\n\tds_read_b32 %0, %3\n\ts_waitcnt lgkmcnt(0)\n\tv_readfirstlane_b32 %1, %0\n\ts_nop 0\n\ts_cmp_ge_u32 %1, %4\n\ts_cbranch_scc1 2f\n\t"
               "s_sleep 1\n\ts_sub_u32 %2, %2, 1\n\ts_cbranch_scc0 1b\n2:"
               : "=&v"(v), "=&s"(sv), "+s"(cnt) : "v"(addr), "s"(target) : "memory", "scc");
}
DI unsigned lds_peek(unsigned addr) { return *(volatile LDS_AS unsigned*)addr; }
DI void lds_check_ge(unsigned v, unsigned addr, unsigned target) {
  unsigned sv, cnt = 1u << 18;
  asm volatile("s_nop 0\n1:\n\tv_readfirstlane_b32 %1, %0\n\ts_cmp_ge_u32 %1, %4\n\ts_cbranch_scc1 2f\n\t"
               "v_mov_b32 %0, %3\n\ts_sleep 1\n\tds_read_b32 %0, %0\n\ts_sub_u32 %2, %2, 1\n\ts_waitcnt lgkmcnt(0)\n\ts_cbranch_scc0 1b\n2:"
               : "+v"(v), "=&s"(sv), "+s"(cnt) : "s"(addr), "s"(target) : "memory", "scc");
}
DI void lds_check_zero(unsigned v, unsigned addr) {
  unsigned sv, cnt = 1u << 18;
  asm volatile("s_nop 0\n1:\n\tv_readfirstlane_b32 %1, %0\n\ts_cmp_eq_u32 %1, 0\n\ts_cbranch_scc1 2f\n\t"
               "v_mov_b32 %0, %3\n\ts_sleep 2\n\tds_read_b32 %0, %0\n\ts_sub_u32 %2, %2, 1\n\ts_waitcnt lgkmcnt(0)\n\ts_cbranch_scc0 1b\n2:"
               : "+v"(v), "=&s"(sv), "+s"(cnt) : "s"(addr) : "memory", "scc");
}

#define STAMP(i) do {} while (0)

template <int SH> DI unsigned src_lane(unsigned lane) { return (((lane >> 2) & 3) << SH) + (lane >> 4) * 64 + (lane & 3) * 16; }
DI unsigned rd_rows(unsigned lane) { const unsigned g = lane >> 4, m = lane & 15; return (m >> 2) * 1024 + (m & 3) * 64 + ((g ^ ((m >> 2) & 2)) << 4); }
DI unsigned rd_cols(unsigned lane) { const unsigned g = lane >> 4, q = (lane >> 2) & 3, pp = lane & 3; return g * 1024 + q * 64 + ((pp ^ (g & 1)) << 4) + 8 * (pp & 1); }

struct Frag { bf16x4 lo[8], hi[8]; bf16x8 b[3]; };
struct GemmCfg { u32x4 srd; unsigned wv[4]; unsigned ring, ga0, ga1, bl, rot, gate; };
template <int SH> DI void gemm_cfg(GemmCfg& G, const char* w, unsigned ring, unsigned bl, unsigned rot, unsigned gate, unsigned lane) {
  constexpr unsigned U = (8u << SH) - 1024u;
  const unsigned w0 = src_lane<SH>(lane);
  G.srd = make_srd(w, (511u << SH) + 256u);
  G.wv[0] = w0; G.wv[1] = (w0 ^ 16u) + U; G.wv[2] = w0 + 2 * U; G.wv[3] = (w0 ^ 16u) + 3 * U;
  G.ring = ring; G.ga0 = rd_cols(lane) + ring; G.ga1 = G.ga0 ^ 8u; G.bl = bl; G.rot = rot; G.gate = gate;
}
template <int SH> DI unsigned w_off(unsigned s, unsigned t, unsigned rot) { return (((s ^ rot) << 5) + 4u * t) << SH; }
template <int SH, int NS> DI void gemm_prime(const GemmCfg& G) {
#pragma unroll
  for (int n = 0; n < NS; ++n) dma4(G.ring + n * 4096, G.srd, G.wv[0], G.wv[1], G.wv[2], G.wv[3], w_off<SH>(n >> 1, n & 1, G.rot));
}
template <int SH, int NS, int BS, int BK, bool GATE, int MODE, int PAR, int XW, class Tail>
DI void gstep(f32x4 (&acc)[8][3], const Frag& c, Frag& n, const GemmCfg& G, unsigned s, Tail tail) {
#define M(k) do { if (MODE != 0) acc[4 * t + (k) / 3][(k) % 3] = mfma(cat(c.lo[4 * t + (k) / 3], c.hi[4 * t + (k) / 3]), c.b[(k) % 3], acc[4 * t + (k) / 3][(k) % 3]); SBAR(); } while (0)
#define RD(i) d[i] = tr_read((((i) & 1) ? G.ga1 : G.ga0) + so_ + ((i) >> 1) * 256)
#pragma unroll
  for (int t = 0; t < 2; ++t) {
    bf16x4* d = t ? n.hi : n.lo;
    const unsigned so_ = ((NS == 4 ? 2 * PAR : 0) + t) * 4096;
    if (MODE == 2 && NS == 4 && t == 1) wait_vm<8>(); else wait_vm<4 * (NS - 1) + (MODE == 0 ? XW : 0)>();
    SBAR();
    M(0); RD(0); RD(1); RD(2); SBAR();
    M(1); RD(3); RD(4); RD(5); SBAR();
    M(2); RD(6); RD(7); SBAR();
    M(3);
    if (t == 1) {
      const unsigned se = s ^ G.rot;
      if (GATE && PAR == 0) { if ((s & 3) == 0) lds_wait_ge(G.gate + (se >> 2) * 4, 1); }
      const unsigned xb = G.bl + ((se & 3) * BS + (se >> 2) * BK);
      n.b[0] = ld128(xb); n.b[1] = ld128(xb + 4096); n.b[2] = ld128(xb + 8192);
    }
    SBAR();
    M(4); SBAR();
    unsigned so = w_off<SH>((s + NS / 2) & 15u, t, G.rot); u32x4 srd = G.srd;
    if (NS == 4 && MODE == 1 && PAR == 0) srd[2] = s < 14 ? srd[2] : 0u;
    M(5); if (MODE != 2) asm volatile("" : "+s"(so)); SBAR();
    M(6); if (MODE != 2 || NS == 2) set_m0(G.ring + so_); SBAR();
    M(7); wait_lgkm0(); if (MODE != 2) dma<0>(srd, G.wv[0], so); else tail(t, 0); SBAR();
    M(8); if (MODE != 2) dma<1024>(srd, G.wv[1], so); else tail(t, 1); SBAR();
    M(9); if (MODE != 2) dma<2048>(srd, G.wv[2], so); else tail(t, 2); SBAR();
    M(10); if (MODE != 2) dma<3072>(srd, G.wv[3], so); else tail(t, 3); SBAR();
    M(11);
  }
#undef M
#undef RD
}
template <int SH, int NS, int BS, int BK, bool GATE, int XW, class Tail, class Mid>
DI void gemm(f32x4 (&acc)[8][3], const GemmCfg& G, Tail tail, Mid mid) {
#pragma unroll
  for (int a = 0; a < 8; ++a)
#pragma unroll
    for (int q = 0; q < 3; ++q) acc[a][q] = f32x4{0.f, 0.f, 0.f, 0.f};
  Frag X, Y;
  gstep<SH, NS, BS, BK, GATE, 0, 0, XW>(acc, Y, X, G, 0, tail);
  mid();
#pragma unroll 1
  for (unsigned s = 1; s < 15; s += 2) {
    gstep<SH, NS, BS, BK, GATE, 1, 1, 0>(acc, X, Y, G, s, tail);
    gstep<SH, NS, BS, BK, GATE, 1, 0, 0>(acc, Y, X, G, s + 1, tail);
  }
  gstep<SH, NS, BS, BK, GATE, 2, 1, 0>(acc, X, Y, G, 15, tail);
#pragma unroll
  for (int i = 0; i < 8; ++i)
#pragma unroll
    for (int q = 0; q < 3; ++q) acc[i][q] = mfma(cat(Y.lo[i], Y.hi[i]), Y.b[q], acc[i][q]);
}

template <class F, int... Ms> DI void for_seq(F& f, std::integer_sequence<int, Ms...>) { (f(std::integral_constant<int, Ms>{}), ...); }
struct Att { bf16x8 q[4][3]; f32x4 o[8][3]; f32x4 l[3]; f32x4 nm[3]; bf16x8 kf[8]; u32x4 ones; };

template <class Hook>
DI void att_chunk(Att& A, float floorv, float thr, unsigned va0, unsigned va1, unsigned ka, Hook hook) {
  f32x4 s[3][2]; u32x4 pw[3]; bf16x4 vlo[8], vhi[8]; float mx[3], ex[3][8];
  auto mm = [&](auto mc) {
    constexpr int m = decltype(mc)::value;
    if constexpr (m < 24) { constexpr int q = m >> 3, kt = (m >> 2) & 1, i = m & 3; s[q][kt] = mfma(A.kf[4 * kt + i], A.q[i][q], i == 0 ? A.nm[q] : s[q][kt]); }
    else { constexpr int q = (m - 24) / 9, i = (m - 24) % 9;
      if constexpr (i == 0) A.l[q] = mfma(__builtin_bit_cast(bf16x8, A.ones), __builtin_bit_cast(bf16x8, pw[q]), A.l[q]);
      else A.o[i - 1][q] = mfma(cat(vlo[i - 1], vhi[i - 1]), __builtin_bit_cast(bf16x8, pw[q]), A.o[i - 1][q]); }
    SBAR();
  };
  auto X = [&](auto qc, auto ic) { constexpr int q = decltype(qc)::value, i = decltype(ic)::value;
    mx[q] = fmaxf(fmaxf(i == 0 ? floorv : mx[q], s[q][i >> 1][2 * (i & 1)]), s[q][i >> 1][2 * (i & 1) + 1]); asm volatile("" : "+v"(mx[q])); };
  auto XB = [&](auto qc) { constexpr int q = decltype(qc)::value;
    if (__builtin_expect(__any(mx[q] > thr), 0)) {
      const float d = xmax16_32(mx[q]);
      const float f = __builtin_amdgcn_exp2f(fminf(-d, 0.f));
#pragma unroll
      for (int r = 0; r < 4; ++r) { s[q][0][r] -= d; s[q][1][r] -= d; A.nm[q][r] -= d; A.l[q][r] *= f; }
#pragma unroll
      for (int i = 0; i < 8; ++i) A.o[i][q] *= f;
    } };
  auto E = [&](auto qc, auto ic) { constexpr int q = decltype(qc)::value, i = decltype(ic)::value;
    ex[q][i] = __builtin_amdgcn_exp2f(s[q][i >> 2][i & 3]); asm volatile("" : "+v"(ex[q][i])); };
  auto C = [&](auto qc, auto ic) { constexpr int q = decltype(qc)::value, i = decltype(ic)::value;
    unsigned wd = cvtpk(ex[q][2 * i], ex[q][2 * i + 1]); asm volatile("" : "+v"(wd)); pw[q][i] = wd; };
#define IC(n) std::integral_constant<int, (n)>{}
  auto gap = [&](auto mc) {
    constexpr int m = decltype(mc)::value;
    mm(mc);
    hook(mc);
    if constexpr (m >= 16 && m < 24) vlo[m - 16] = tr_read((((m - 16) & 1) ? va1 : va0) + ((m - 16) >> 1) * 256);
    if constexpr (m >= 43) A.kf[m - 43] = ld128(ka + ((m - 43) >> 2) * 4096 + ((m - 43) & 3) * 256);
    SBAR();
    if constexpr (m >= 12 && m < 20) E(IC(0), IC(m - 12));
    if constexpr (m >= 22 && m < 30) E(IC(1), IC(m - 22));
    if constexpr (m >= 32 && m < 40) E(IC(2), IC(m - 32));
    SBAR();
    if constexpr (m >= 16 && m < 24) vhi[m - 16] = tr_read((((m - 16) & 1) ? va1 : va0) + ((m - 16) >> 1) * 256 + 4096);
    SBAR();
    if constexpr (m == 8) { X(IC(0), IC(0)); X(IC(0), IC(1)); }
    if constexpr (m == 10) { X(IC(0), IC(2)); X(IC(0), IC(3)); }
    if constexpr (m == 11) XB(IC(0));
    if constexpr (m == 14 || m == 16 || m == 18 || m == 20) C(IC(0), IC((m - 14) / 2));
    if constexpr (m == 15 || m == 17 || m == 19) X(IC(1), IC((m - 15) / 2));
    if constexpr (m == 20) X(IC(1), IC(3));
    if constexpr (m == 21) XB(IC(1));
    if constexpr (m == 24 || m == 26 || m == 28 || m == 30) C(IC(1), IC((m - 24) / 2));
    if constexpr (m == 23 || m == 25 || m == 27 || m == 29) X(IC(2), IC((m - 23) / 2));
    if constexpr (m == 31) XB(IC(2));
    if constexpr (m == 34 || m == 36 || m == 38 || m == 40) C(IC(2), IC((m - 34) / 2));
    SBAR();
  };
  for_seq(gap, std::make_integer_sequence<int, 51>{});
}

DI void att_init(Att& A, f32x4 (&acc)[8][3], bool odd) {
#pragma unroll
  for (int a = 0; a < 4; ++a)
#pragma unroll
    for (int q = 0; q < 3; ++q) {
      A.q[a][q] = __builtin_bit_cast(bf16x8, pack_pair(acc[2 * a][q], acc[2 * a + 1][q], QSCALE, odd));
      A.o[2 * a][q] = f32x4{0.f, 0.f, 0.f, 0.f}; A.o[2 * a + 1][q] = f32x4{0.f, 0.f, 0.f, 0.f};
    }
#pragma unroll
  for (int q = 0; q < 3; ++q) { A.l[q] = f32x4{0.f, 0.f, 0.f, 0.f}; A.nm[q] = f32x4{0.f, 0.f, 0.f, 0.f}; }
  A.ones = u32x4{0x3f803f80u, 0x3f803f80u, 0x3f803f80u, 0x3f803f80u}; asm volatile("" : "+v"(A.ones));
}
DI void att_read_k(Att& A, unsigned ka) {
#pragma unroll
  for (int i = 0; i < 8; ++i) A.kf[i] = ld128(ka + (i >> 2) * 4096 + (i & 3) * 256);
}
DI void att_store_o(Att& A, unsigned obuf_lane, bool odd) {
#pragma unroll
  for (int q = 0; q < 3; ++q) {
    const float inv = __builtin_amdgcn_rcpf(A.l[q][0]);
#pragma unroll
    for (int a = 0; a < 4; ++a) *(LDS_AS u32x4*)(obuf_lane + q * 4096 + a * 1024) = pack_pair(A.o[2 * a][q], A.o[2 * a + 1][q], inv, odd);
  }
  wait_lgkm0();
}
DI void store_part(f32x4 (&acc)[8][3], char* dst, bool odd) {
#pragma unroll
  for (int a = 0; a < 4; ++a)
#pragma unroll
    for (int q = 0; q < 3; ++q) *(u32x4*)(dst + (a * 3 + q) * 1024) = pack_pair(acc[2 * a][q], acc[2 * a + 1][q], 1.0f, odd);
}
DI void load_x(const char* x, unsigned kq, unsigned lane) {
  const u32x4 sx = make_srd(x, 49152);
  const unsigned v = src_lane<10>(lane), v1 = v + 3072, v2 = (v ^ 32u) + 6144, v3 = (v ^ 32u) + 9216;
#pragma unroll
  for (int i = 0; i < 3; ++i) dma4<0>(RX + kq * 12288 + i * 4096, sx, v, v1, v2, v3, kq * 256 + i * 16384);
}

__global__ __launch_bounds__(512, 2) void fused_k(Params p) {
  __shared__ __attribute__((aligned(1024))) char smem[LDS_BYTES];
  asm volatile("" :: "v"((LDS_AS char*)smem));
  const unsigned tid = threadIdx.x;
  unsigned lane;
#define NEWLANE() (lane = fresh_lane())
#define ODD ((bool)((lane >> 4) & 1))
  const unsigned wid = __builtin_amdgcn_readfirstlane(tid >> 6), w = wid & 3;
  const bool isB = wid < 4;
  const unsigned j = blockIdx.x & 7, b = blockIdx.x >> 3, h = 4 * j + w, rot = b & 15;
  STAMP(0);
  if (tid < 64) *(LDS_AS unsigned*)(RS + tid * 4) = 0;
  f32x4 acc[8][3];
  Att A;
  GemmCfg G;
  const float ninf = -__builtin_inff();
  if (isB) {
    const unsigned ring = RB + w * 16384;
    NEWLANE();
    u32x4 sk = make_srd(p.k[1] + (size_t)(b * 32 + h) * 131072, 131072), sv = make_srd(p.v[1] + (size_t)(b * 32 + h) * 131072, 131072);
    const unsigned s0 = src_lane<8>(lane), s16 = s0 ^ 16u, s32 = s0 ^ 32u;
    gemm_cfg<13>(G, p.wq[1] + h * 256, ring + 8192, rd_rows(lane) + RX, rot, W_XB, lane);
    gemm_prime<13, 2>(G);
    dma4(ring, sk, s0, s0, s32, s32, 0); dma4(ring + 4096, sk, s0, s0, s32, s32, 4096);
    wait_lgkm0();
    __builtin_amdgcn_s_barrier();
    STAMP(1);
    gemm<13, 2, 256, 12288, true, 8>(acc, G, [&](int t, int u) {
      if (u == 0) dma<0>(sv, s0, t * 4096); else if (u == 1) dma<1024>(sv, s16, t * 4096); else if (u == 2) dma<2048>(sv, s0, t * 4096); else dma<3072>(sv, s16, t * 4096); }, [&] { STAMP(5); });
    lds_add(W_BQP, 1);
    STAMP(2);
    NEWLANE();
    const unsigned ka = rd_rows(lane) + ring, va0 = rd_cols(lane) + ring + 8192, va1 = va0 ^ 8u;
    att_read_k(A, ka);
    att_init(A, acc, ODD);
    float floorv = ninf, thr = ninf;
#pragma unroll 1
    for (unsigned c = 0; c < 16; ++c) {
      unsigned so = c * 8192 + 8192, so2 = so + 4096, pz = 0;
      if (c == 15) { sk[2] = 0; sv[2] = 0; }
      att_chunk(A, floorv, thr, va0, va1, ka, [&](auto mc) {
        constexpr int m = decltype(mc)::value;
        if constexpr (PAUSE && (m == 0 || m == 24)) pz = lds_peek(W_PAUSE);
        if constexpr (m == 3) set_m0(ring);
        if constexpr (m == 4) { wait_lgkm0(); if (PAUSE) lds_check_zero(pz, W_PAUSE); dma<0>(sk, s0, so); }
        if constexpr (m == 5) dma<1024>(sk, s0, so);
        if constexpr (m == 6) dma<2048>(sk, s32, so);
        if constexpr (m == 7) { dma<3072>(sk, s32, so); set_m0(ring + 4096); }
        if constexpr (m == 8) dma<0>(sk, s0, so2);
        if constexpr (m == 9) dma<1024>(sk, s0, so2);
        if constexpr (m == 10) dma<2048>(sk, s32, so2);
        if constexpr (m == 11) dma<3072>(sk, s32, so2);
        if constexpr (m == 16 || m == 43) wait_vm<8>();
        if constexpr (m == 27) set_m0(ring + 8192);
        if constexpr (m == 28) { wait_lgkm0(); if (PAUSE) lds_check_zero(pz, W_PAUSE); dma<0>(sv, s0, so); }
        if constexpr (m == 29) dma<1024>(sv, s16, so);
        if constexpr (m == 30) dma<2048>(sv, s0, so);
        if constexpr (m == 31) { dma<3072>(sv, s16, so); set_m0(ring + 12288); }
        if constexpr (m == 32) dma<0>(sv, s0, so2);
        if constexpr (m == 33) dma<1024>(sv, s16, so2);
        if constexpr (m == 34) dma<2048>(sv, s0, so2);
        if constexpr (m == 35) dma<3072>(sv, s16, so2);
        if constexpr (m == 41) lds_add(W_BPROG, 1);
      });
      floorv = 0.f; thr = THR;
    }
    STAMP(3);
    wait_vm<0>(); wait_lgkm0();
    NEWLANE();
    att_store_o(A, ring + lane * 16, ODD);
    lds_add(W_OB + 4 * w, 1);
    STAMP(4);
  } else {
    if (APRIO) __builtin_amdgcn_s_setprio(1);
    NEWLANE();
    { const unsigned kq = w ^ (rot >> 2);
      load_x(p.x[1] + (size_t)b * 49152, kq, lane);
      wait_lgkm0();
      __builtin_amdgcn_s_barrier();
      wait_vm<0>();
      lds_add(W_XB + 4 * kq, 1); }
    STAMP(1);
    lds_wait_ge(W_BQP, 4);
    load_x(p.x[0] + (size_t)b * 49152, w, lane);
    { const char* s0 = p.wq[0] + (size_t)(16 * b + 4 * w) * 8192 + j * 1024 + lane * 16;
      const char* s1 = p.wo[0] + (size_t)(512 * j + 16 * b + 4 * w) * 1024 + lane * 16;
      const char* s2 = p.wo[1] + (size_t)(512 * j + 16 * b + 4 * w) * 1024 + lane * 16;
      u32x4 t = {0, 0, 0, 0};
#pragma unroll
      for (int i = 0; i < 4; ++i) {
        asm volatile("global_load_dwordx4 %0, %1, off nt" : "+v"(t) : "v"(s0 + i * 8192) : "memory");
        asm volatile("global_load_dwordx4 %0, %1, off offset:%2 nt" : "+v"(t) : "v"(s1), "i"(i * 1024) : "memory");
        asm volatile("global_load_dwordx4 %0, %1, off offset:%2 nt" : "+v"(t) : "v"(s2), "i"(i * 1024) : "memory");
      }
      asm volatile("s_waitcnt vmcnt(0)" : "+v"(t) :: "memory");
#pragma unroll
      for (int i = 0; i < 4; ++i) {
        asm volatile("global_load_dwordx4 %0, %1, off" : "+v"(t) : "v"(s0 + i * 8192) : "memory");
        asm volatile("global_load_dwordx4 %0, %1, off offset:%2" : "+v"(t) : "v"(s1), "i"(i * 1024) : "memory");
        asm volatile("global_load_dwordx4 %0, %1, off offset:%2" : "+v"(t) : "v"(s2), "i"(i * 1024) : "memory");
      }
      asm volatile("s_waitcnt vmcnt(0)" : "+v"(t) :: "memory"); }
    unsigned bar = 4;
    lds_add(W_ABAR, 1);
    lds_wait_ge(W_BPROG, GATE0 + (GATEK * b) / 32);
    lds_wait_ge(W_ABAR, bar);
    STAMP(2);
    const unsigned ring = RA + w * 8192;
    auto notail = [&](int t, int u) { if (t == 0 && u == 0) wait_vm<0>(); };
    lds_add(W_PAUSE, 1);
    NEWLANE();
    gemm_cfg<13>(G, p.wq[0] + h * 256, ring, rd_rows(lane) + RX, rot, 0, lane);
    gemm_prime<13, 2>(G);
    gemm<13, 2, 256, 12288, false, 0>(acc, G, notail, [&] { STAMP(9); });
    lds_add(W_PAUSE, ~0u);
    lds_add(W_ABAR, 1); bar += 4;
    STAMP(3);
    NEWLANE();
    u32x4 skv = make_srd((w < 2 ? p.k[0] : p.v[0]) + (size_t)(b * 8 + j) * 131072 + (w & 1) * 4096, 131072 - 4096);
    const unsigned s0 = src_lane<8>(lane), s1 = s0 ^ (w < 2 ? 0u : 16u), s2 = s0 ^ (w < 2 ? 32u : 0u), s3 = s0 ^ (w < 2 ? 32u : 16u);
    lds_wait_ge(W_ABAR, bar);
#pragma unroll
    for (int c = 0; c < NSA; ++c) dma4(RX + c * 16384 + w * 4096, skv, s0, s1, s2, s3, c * 8192);
    att_init(A, acc, ODD);
    wait_vm<4 * (NSA - 1)>();
    lds_add(W_ABAR, 1); bar += 4;
    lds_wait_ge(W_ABAR, bar);
    const unsigned kl = rd_rows(lane) + RX, vl = rd_cols(lane) + RX + 8192;
    att_read_k(A, kl);
    float floorv = ninf, thr = ninf;
    unsigned cur = 0;
#pragma unroll 1
    for (unsigned c = 0; c < 16; ++c) {
      const unsigned nxt = cur == (NSA - 1) * 16384 ? 0u : cur + 16384, so = (c + NSA) * 8192;
      const unsigned va0 = vl + cur, va1 = va0 ^ 8u, ka = kl + nxt;
      unsigned bz = 0;
      bar += 4;
      if (c + NSA == 16) skv[2] = 0;
      att_chunk(A, floorv, thr, va0, va1, ka, [&](auto mc) {
        constexpr int m = decltype(mc)::value;
        if constexpr (m == 35) { wait_vm<4 * (NSA - 2)>(); wait_lgkm0(); lds_add(W_ABAR, 1); }
        if constexpr (m == 38) bz = lds_peek(W_ABAR);
        if constexpr (m == 41) set_m0(RX + cur + w * 4096);
        if constexpr (m == 42) lds_check_ge(bz, W_ABAR, bar);
        if constexpr (m == 43) dma<0>(skv, s0, so);
        if constexpr (m == 45) dma<1024>(skv, s1, so);
        if constexpr (m == 47) dma<2048>(skv, s2, so);
        if constexpr (m == 49) dma<3072>(skv, s3, so);
      });
      floorv = 0.f; thr = THR; cur = nxt;
    }
    STAMP(4);
    wait_vm<0>(); wait_lgkm0();
    lds_add(W_ABAR, 1); bar += 4;
    lds_add(W_PAUSE, 1);
    NEWLANE();
    gemm_cfg<10>(G, p.wo[0] + (size_t)j * 524288 + w * 256, ring, RX + lane * 16, 0, 0, lane);
    lds_wait_ge(W_ABAR, bar);
    gemm_prime<10, 2>(G);
    att_store_o(A, RX + w * 12288 + lane * 16, ODD);
    lds_add(W_ABAR, 1); bar += 4;
    lds_wait_ge(W_ABAR, bar);
    gemm<10, 2, 1024, 12288, false, 0>(acc, G, notail, [&] { STAMP(10); });
    lds_add(W_PAUSE, ~0u);
    lds_add(W_ABAR, 1); bar += 4;
    STAMP(5);
    NEWLANE();
    char* part = p.part + (size_t)((b * 8 + j) * 4 + w) * 12288;
    store_part(acc, part + lane * 16, ODD);
    gemm_cfg<10>(G, p.wo[1] + (size_t)j * 524288 + w * 256, RX + w * 16384, RB + lane * 16, 0, W_OB, lane);
    lds_wait_ge(W_ABAR, bar);
    gemm_prime<10, 4>(G);
    STAMP(6);
    gemm<10, 4, 1024, 16384, true, 0>(acc, G, [](int, int) {}, [&] { STAMP(11); });
    STAMP(7);
    NEWLANE();
    store_part(acc, part + (size_t)256 * 49152 + lane * 16, ODD);
    wait_vm<0>();
    STAMP(8);
  }
}

__global__ __launch_bounds__(256) void reduce_k(const char* part, char* out) {
  const unsigned t = blockIdx.x * 256 + threadIdx.x, lane = t & 63, f = t >> 6;
  const unsigned fr = f % 48, bb = f / 48, br = bb >> 5, b = bb & 31, w = fr / 12, a = (fr % 12) / 3, q = fr % 3;
  const char* src = part + (size_t)(br * 256 + b * 8) * 49152 + fr * 1024 + lane * 16;
  u32x4 v[8];
#pragma unroll
  for (int jj = 0; jj < 8; ++jj) v[jj] = *(const u32x4*)(src + (size_t)jj * 49152);
  float s[8] = {0, 0, 0, 0, 0, 0, 0, 0};
#pragma unroll
  for (int jj = 0; jj < 8; ++jj)
#pragma unroll
    for (int i = 0; i < 4; ++i) { s[2 * i] += __uint_as_float(v[jj][i] << 16); s[2 * i + 1] += __uint_as_float(v[jj][i] & 0xffff0000u); }
  u32x4 r = {cvtpk(s[0], s[1]), cvtpk(s[2], s[3]), cvtpk(s[4], s[5]), cvtpk(s[6], s[7])};
  *(u32x4*)(out + (size_t)(b * 48 + q * 16 + (lane & 15)) * 2048 + br * 1024 + w * 256 + a * 64 + (lane >> 4) * 16) = r;
}

extern "C" void kernel_launch(void* const* d_in, const int* in_sizes, int n_in, void* d_out, int out_size, void* d_ws, size_t ws_size, hipStream_t stream) {
  Params p;
  for (int br = 0; br < 2; ++br) {
    p.x[br] = (const char*)d_in[br * 5 + 0]; p.k[br] = (const char*)d_in[br * 5 + 1]; p.v[br] = (const char*)d_in[br * 5 + 2];
    p.wq[br] = (const char*)d_in[br * 5 + 3]; p.wo[br] = (const char*)d_in[br * 5 + 4];
  }
  p.part = (char*)d_ws; p.stamps = (u64*)((char*)d_ws + (64u << 20));
  fused_k<<<256, 512, 0, stream>>>(p);
  reduce_k<<<768, 256, 0, stream>>>((const char*)d_ws, (char*)d_out);
}
```

```cpp
#include <hip/hip_runtime.h>
#include <stdint.h>
#include <utility>

typedef __bf16 bf16x8 __attribute__((ext_vector_type(8)));
typedef __bf16 bf16x4 __attribute__((ext_vector_type(4)));
typedef __bf16 bf16x2 __attribute__((ext_vector_type(2)));
typedef float f32x4 __attribute__((ext_vector_type(4)));
typedef float f32x2 __attribute__((ext_vector_type(2)));
typedef unsigned u32x4 __attribute__((ext_vector_type(4)));
typedef unsigned long long u64;
#define LDS_AS __attribute__((address_space(3)))
#define DI __device__ __forceinline__
#define SBAR() __builtin_amdgcn_sched_barrier(0)

#define STAMPS 0
#define KNOB(n, v) constexpr unsigned n = (v)
#define K_TQ0 1100
#define K_DQ 45
#define K_TO0 3000
#define K_DO 45
#define K_HM 12u
#define K_BD 16
#define K_P0 50
#define K_P1 50
#define K_P2 0
#define K_P3 0
#define K_TG2 150
#define K_PW 5
#define K_TLB 1
#define K_CA 0
#define K_CB 0
#define K_OROT 1
#define K_TS 0
#define K_PC0 10
#define K_PD 110
#define K_TW 0
#define K_HOM 1
#define K_WL 3
#define K_TN0 0
#define K_TN1 0
#define K_QBNT 0
#define K_ARR 30
#define K_OBP 1
#define K_G2 1
#define K_XB 0
#define K_XA 1
#define K_PNT 1
#define K_WNT 0
#define K_STAUX 16
#define K_NORED 0
#define APRIO 1
#define NSA 4

constexpr unsigned RB = 0;
constexpr unsigned RX = 65536;
constexpr unsigned RA = 114688;
constexpr unsigned RDUMMY = 155648;
constexpr unsigned RS = 163584, LDS_BYTES = 163840;
constexpr unsigned W_ABAR = RS + 0, W_GO = RS + 16, W_BQP = RS + 32, W_PAUSE = RS + 48, W_OB = RS + 64  , W_XB = RS + 96  , W_TOKV = RS + 112, W_PARK = RS + 144  , W_BDONE = RS + 160, W_WDONE = RS + 176, W_TOK = RS + 128  ;
constexpr float QSCALE = 0.08838834764831845f * 1.4426950408889634f;
constexpr float BIAS = 7.0f;

struct Params {
  const char* x[2]; const char* k[2]; const char* v[2]; const char* wq[2]; const char* wo[2];
  char* part; u64* flags; char* out; u64* stamps;
};

DI u32x4 make_srd(const char* p, unsigned bytes) { u32x4 s = {(unsigned)(uintptr_t)p, (unsigned)((uintptr_t)p >> 32), bytes, 0x00020000u}; return s; }
template <int N> DI void wait_vm() { asm volatile("s_waitcnt vmcnt(%0)" :: "i"(N) : "memory"); }
DI void wait_lgkm0() { asm volatile("s_waitcnt lgkmcnt(0)" ::: "memory"); }
DI void set_m0(unsigned dst) { asm volatile("s_mov_b32 m0, %0\n\ts_nop 0" :: "s"(dst) : "memory"); }
template <int OFF, int NT = 1> DI void dma(u32x4 srd, unsigned v, unsigned so) {
  if constexpr (NT == 2) asm volatile("buffer_load_dwordx4 %0, %1, %3 offen offset:%2 sc1 lds" :: "v"(v), "s"(srd), "i"(OFF), "s"(so) : "memory");
  else if constexpr (NT) asm volatile("buffer_load_dwordx4 %0, %1, %3 offen offset:%2 nt lds" :: "v"(v), "s"(srd), "i"(OFF), "s"(so) : "memory");
  else asm volatile("buffer_load_dwordx4 %0, %1, %3 offen offset:%2 lds" :: "v"(v), "s"(srd), "i"(OFF), "s"(so) : "memory");
}
template <int NT = 1> DI void dma4(unsigned dst, u32x4 srd, unsigned v0, unsigned v1, unsigned v2, unsigned v3, unsigned so) {
  set_m0(dst); dma<0, NT>(srd, v0, so); dma<1024, NT>(srd, v1, so); dma<2048, NT>(srd, v2, so); dma<3072, NT>(srd, v3, so);
}
DI bf16x4 tr_read(unsigned a) { return __builtin_amdgcn_ds_read_tr16_b64_v4bf16((LDS_AS bf16x4*)a); }
DI bf16x8 ld128(unsigned a) { return *(LDS_AS bf16x8*)a; }
DI bf16x8 cat(bf16x4 a, bf16x4 b) { return __builtin_shufflevector(a, b, 0, 1, 2, 3, 4, 5, 6, 7); }
DI f32x4 mfma(bf16x8 a, bf16x8 b, f32x4 c) { return __builtin_amdgcn_mfma_f32_16x16x32_bf16(a, b, c, 0, 0, 0); }
DI unsigned cvtpk(float lo, float hi) { f32x2 v = {lo, hi}; bf16x2 b = __builtin_convertvector(v, bf16x2); return __builtin_bit_cast(unsigned, b); }
DI u32x4 pack_pair(f32x4 t0, f32x4 t1, float s, bool odd) {
  unsigned a0 = cvtpk(t0[0] * s, t0[1] * s), a1 = cvtpk(t0[2] * s, t0[3] * s), b0 = cvtpk(t1[0] * s, t1[1] * s), b1 = cvtpk(t1[2] * s, t1[3] * s);
  u32x4 r = {odd ? b0 : a0, odd ? b1 : a1, odd ? a0 : b0, odd ? a1 : b1}; return r;
}
DI unsigned fresh_lane() { unsigned l; asm volatile("v_mbcnt_lo_u32_b32 %0, -1, 0\n\tv_mbcnt_hi_u32_b32 %0, -1, %0" : "=v"(l)); return l; }
DI float xmax16_32(float d) {
  auto r = __builtin_amdgcn_permlane16_swap(__float_as_uint(d), __float_as_uint(d), false, false);
  d = fmaxf(__uint_as_float(r[0]), __uint_as_float(r[1]));
  auto u = __builtin_amdgcn_permlane32_swap(__float_as_uint(d), __float_as_uint(d), false, false);
  return fmaxf(__uint_as_float(u[0]), __uint_as_float(u[1]));
}
DI void lds_add(unsigned addr, unsigned val) {
  asm volatile("s_mov_b64 exec, 1\n\tds_add_u32 %0, %1\n\ts_mov_b64 exec, -1" :: "v"(addr), "v"(val) : "memory");
}
DI void lds_wait_ge(unsigned addr, unsigned target) {
  unsigned v, sv, cnt = 1u << 18;
  asm volatile("1:\n\tds_read_b32 %0, %3\n\ts_waitcnt lgkmcnt(0)\n\tv_readfirstlane_b32 %1, %0\n\ts_nop 0\n\ts_cmp_ge_u32 %1, %4\n\ts_cbranch_scc1 2f\n\t"
               "s_sleep 1\n\ts_sub_u32 %2, %2, 1\n\ts_cbranch_scc0 1b\n2:"
               : "=&v"(v), "=&s"(sv), "+s"(cnt) : "v"(addr), "s"(target) : "memory", "scc");
}
DI unsigned lds_peek(unsigned addr) { return *(volatile LDS_AS unsigned*)addr; }
DI unsigned to_sgpr(unsigned v) { unsigned r = __builtin_amdgcn_readfirstlane(v); asm volatile("" : "+s"(r)); return r; }
DI void lds_wait_clear(unsigned addr, unsigned mask) {
  unsigned v, sv, cnt = 1u << 18;
  asm volatile("1:\n\ts_sleep 2\n\tds_read_b32 %0, %3\n\ts_waitcnt lgkmcnt(0)\n\tv_readfirstlane_b32 %1, %0\n\ts_and_b32 %1, %1, %4\n\ts_cbranch_scc0 2f\n\t"
               "s_sub_u32 %2, %2, 1\n\ts_cbranch_scc0 1b\n2:"
               : "=&v"(v), "=&s"(sv), "+s"(cnt) : "v"(addr), "s"(mask) : "memory", "scc");
}
DI unsigned now_ticks() { return (unsigned)__builtin_amdgcn_s_memrealtime(); }
DI void wait_until(unsigned t0, unsigned ticks) {
#pragma unroll 1
  while (now_ticks() - t0 < ticks) __builtin_amdgcn_s_sleep(4);
}

#define STAMP(i) do {} while (0)
#define STAMP_DUMP() do {} while (0)

template <int SH> DI unsigned src_lane(unsigned lane) { return (((lane >> 2) & 3) << SH) + (lane >> 4) * 64 + (lane & 3) * 16; }
DI unsigned rd_rows(unsigned lane) { const unsigned g = lane >> 4, m = lane & 15; return (m >> 2) * 1024 + (m & 3) * 64 + ((g ^ ((m >> 2) & 2)) << 4); }
DI unsigned rd_cols(unsigned lane) { const unsigned g = lane >> 4, q = (lane >> 2) & 3, pp = lane & 3; return g * 1024 + q * 64 + ((pp ^ (g & 1)) << 4) + 8 * (pp & 1); }

struct Frag { bf16x4 lo[8], hi[8]; bf16x8 b[3]; };
struct GemmCfg { u32x4 srd; unsigned wv[4]; unsigned ring, ga0, ga1, bl, rot, gate; };
template <int SH> DI void gemm_cfg(GemmCfg& G, const char* w, unsigned ring, unsigned bl, unsigned rot, unsigned gate, unsigned lane) {
  constexpr unsigned U = (8u << SH) - 1024u;
  const unsigned w0 = src_lane<SH>(lane);
  G.srd = make_srd(w, (511u << SH) + 256u);
  G.wv[0] = w0; G.wv[1] = (w0 ^ 16u) + U; G.wv[2] = w0 + 2 * U; G.wv[3] = (w0 ^ 16u) + 3 * U;
  G.ring = ring; G.ga0 = rd_cols(lane) + ring; G.ga1 = G.ga0 ^ 8u; G.bl = bl; G.rot = rot; G.gate = gate;
}
template <int SH> DI unsigned w_off(unsigned s, unsigned t, unsigned rot) { return (((s ^ rot) << 5) + 4u * t) << SH; }
template <int SH, int NS> DI void gemm_prime(const GemmCfg& G) {
#pragma unroll
  for (int n = 0; n < NS; ++n) dma4(G.ring + n * 4096, G.srd, G.wv[0], G.wv[1], G.wv[2], G.wv[3], w_off<SH>(n >> 1, n & 1, G.rot));
}
template <int SH, int NS, int BS, int BK, int GATE, int MODE, int PAR, int WNT, class Tail>
DI void gstep(f32x4 (&acc)[8][3], const Frag& c, Frag& n, const GemmCfg& G, unsigned s, Tail tail) {
#define M(k) do { if (MODE != 0) acc[4 * t + (k) / 3][(k) % 3] = mfma(cat(c.lo[4 * t + (k) / 3], c.hi[4 * t + (k) / 3]), c.b[(k) % 3], acc[4 * t + (k) / 3][(k) % 3]); SBAR(); } while (0)
#define RD(i) d[i] = tr_read((((i) & 1) ? G.ga1 : G.ga0) + so_ + ((i) >> 1) * 256)
#pragma unroll
  for (int t = 0; t < 2; ++t) {
    bf16x4* d = t ? n.hi : n.lo;
    const unsigned so_ = ((NS == 4 ? 2 * PAR : 0) + t) * 4096;
    if (MODE == 2 && t == 1) { if (NS == 4) wait_vm<8>(); } else wait_vm<4 * (NS - 1)>();
    SBAR();
    M(0); RD(0); RD(1); RD(2); SBAR();
    M(1); RD(3); RD(4); RD(5); SBAR();
    M(2); RD(6); RD(7); SBAR();
    M(3);
    if (t == 1) {
      const unsigned se = s ^ G.rot;
      if (GATE && PAR == 0) { if ((s & 3) == 0) {
        if (GATE == 2 && MODE != 0) lds_add(W_PAUSE, 0u - 256u);
        lds_wait_ge(G.gate + (se >> 2) * 4, SH == 13 ? 4 : 1);
        if (GATE == 2) lds_add(W_PAUSE, 256u); } }
      const unsigned xb = G.bl + ((se & 3) * BS + (se >> 2) * BK);
      n.b[0] = ld128(xb); n.b[1] = ld128(xb + 4096); n.b[2] = ld128(xb + 8192);
    }
    SBAR();
    M(4); SBAR();
    unsigned so = w_off<SH>((s + NS / 2) & 15u, t, G.rot); u32x4 srd = G.srd;
    if (NS == 4 && MODE == 1 && PAR == 0) srd[2] = s < 14 ? srd[2] : 0u;
    M(5); if (MODE != 2) asm volatile("" : "+s"(so)); SBAR();
    M(6); if (MODE != 2) set_m0(G.ring + so_); SBAR();
    M(7); wait_lgkm0(); if (MODE != 2) dma<0, WNT>(srd, G.wv[0], so); else { if (NS == 2 && t == 0) wait_vm<0>(); tail(4 * t); } SBAR();
    M(8); if (MODE != 2) dma<1024, WNT>(srd, G.wv[1], so); else tail(4 * t + 1); SBAR();
    M(9); if (MODE != 2) dma<2048, WNT>(srd, G.wv[2], so); else tail(4 * t + 2); SBAR();
    M(10); if (MODE != 2) dma<3072, WNT>(srd, G.wv[3], so); else tail(4 * t + 3); SBAR();
    M(11);
  }
#undef M
#undef RD
}
template <int SH, int NS, int BS, int BK, int GATE, int WNT, class Tail, class Mid>
DI void gemm(f32x4 (&acc)[8][3], const GemmCfg& G, Tail tail, Mid mid) {
#pragma unroll
  for (int a = 0; a < 8; ++a)
#pragma unroll
    for (int q = 0; q < 3; ++q) acc[a][q] = f32x4{0.f, 0.f, 0.f, 0.f};
  Frag X, Y;
  gstep<SH, NS, BS, BK, GATE, 0, 0, WNT>(acc, Y, X, G, 0, tail);
  mid();
#pragma unroll 1
  for (unsigned s = 1; s < 15; s += 2) {
    gstep<SH, NS, BS, BK, GATE, 1, 1, WNT>(acc, X, Y, G, s, tail);
    gstep<SH, NS, BS, BK, GATE, 1, 0, WNT>(acc, Y, X, G, s + 1, tail);
  }
  gstep<SH, NS, BS, BK, GATE, 2, 1, WNT>(acc, X, Y, G, 15, tail);
#pragma unroll
  for (int i = 0; i < 8; ++i)
#pragma unroll
    for (int q = 0; q < 3; ++q) acc[i][q] = mfma(cat(Y.lo[i], Y.hi[i]), Y.b[q], acc[i][q]);
}

template <class F, int... Ms> DI void for_seq(F& f, std::integer_sequence<int, Ms...>) { (f(std::integral_constant<int, Ms>{}), ...); }
struct Att { bf16x8 q[4][3]; f32x4 o[8][3]; f32x4 l[3]; f32x4 nm[3]; bf16x8 kf[8]; u32x4 ones; };

template <int N> DI void wait_lgkm() { __builtin_amdgcn_s_waitcnt(0xC07F | (N << 8)); }
template <class Hook>
DI void att_chunk(Att& A, float floorv, unsigned force, unsigned va0, unsigned va1, unsigned ka, Hook hook) {
  f32x4 s[3][2]; u32x4 pw[3]; bf16x4 vlo[8], vhi[8]; float ex[3][8]; unsigned tt[3]; u64 big[3];
#define IC(n) std::integral_constant<int, (n)>{}
  auto mm = [&](auto mc) {
    constexpr int m = decltype(mc)::value;
    if constexpr (m < 24) { constexpr int q = m >> 3, kt = (m >> 2) & 1, i = m & 3; s[q][kt] = mfma(A.kf[4 * kt + i], A.q[i][q], i == 0 ? A.nm[q] : s[q][kt]); }
    else { constexpr int q = (m - 24) / 9, i = (m - 24) % 9;
      if constexpr (i == 0) A.l[q] = mfma(__builtin_bit_cast(bf16x8, A.ones), __builtin_bit_cast(bf16x8, pw[q]), A.l[q]);
      else A.o[i - 1][q] = mfma(cat(vlo[i - 1], vhi[i - 1]), __builtin_bit_cast(bf16x8, pw[q]), A.o[i - 1][q]); }
    SBAR();
  };
  auto E = [&](auto qc, auto ic) { constexpr int q = decltype(qc)::value, i = decltype(ic)::value;
    ex[q][i] = __builtin_amdgcn_exp2f(s[q][i >> 2][i & 3]); asm volatile("" :: "v"(ex[q][i])); SBAR(); };
  auto C = [&](auto qc, auto ic) { constexpr int q = decltype(qc)::value, i = decltype(ic)::value;
    const unsigned wd = cvtpk(ex[q][2 * i], ex[q][2 * i + 1]); asm volatile("" :: "v"(wd)); pw[q][i] = wd; SBAR(); };
  auto T1 = [&](auto qc) { constexpr int q = decltype(qc)::value; tt[q] = pw[q][0] | pw[q][1] | pw[q][2]; asm volatile("" : "+v"(tt[q])); SBAR(); };
  auto T2 = [&](auto qc) { constexpr int q = decltype(qc)::value; big[q] = __ballot(((tt[q] | pw[q][3]) & 0x40004000u) >= force); asm volatile("" : "+s"(big[q])); SBAR(); };
  auto BR = [&](auto qc) { constexpr int q = decltype(qc)::value;
    if (__builtin_expect(big[q] != 0, 0)) {
      float d = fmaxf(fmaxf(fmaxf(s[q][0][0], s[q][0][1]), fmaxf(s[q][0][2], s[q][0][3])), fmaxf(fmaxf(s[q][1][0], s[q][1][1]), fmaxf(s[q][1][2], s[q][1][3])));
      d = fmaxf(xmax16_32(d) + BIAS, floorv);
      const float f = __builtin_amdgcn_exp2f(fminf(-d, 0.f));
#pragma unroll
      for (int r = 0; r < 4; ++r) { A.nm[q][r] -= d; A.l[q][r] *= f;
        pw[q][r] = cvtpk(__builtin_amdgcn_exp2f(s[q][r >> 1][2 * (r & 1)] - d), __builtin_amdgcn_exp2f(s[q][r >> 1][2 * (r & 1) + 1] - d)); }
#pragma unroll
      for (int i = 0; i < 8; ++i) A.o[i][q] *= f;
    }
    SBAR(); };
  auto gap = [&](auto mc) {
    constexpr int m = decltype(mc)::value;
    if constexpr (m == 0) wait_lgkm<4>();
    if constexpr (m == 4 || m == 29) wait_lgkm<0>();
    if constexpr (m == 25) wait_lgkm<8>();
    SBAR();
    mm(mc);
    hook(mc);
    SBAR();
    if constexpr (m >= 16 && m < 24) { vlo[m - 16] = tr_read((((m - 16) & 1) ? va1 : va0) + ((m - 16) >> 1) * 256);
                                       vhi[m - 16] = tr_read((((m - 16) & 1) ? va1 : va0) + ((m - 16) >> 1) * 256 + 4096); }
    if constexpr (m >= 43) A.kf[m - 43] = ld128(ka + ((m - 43) >> 2) * 4096 + ((m - 43) & 3) * 256);
    SBAR();
    if constexpr (m >= 6 && m < 14) E(IC(0), IC(m - 6));
    if constexpr (m == 14 || m == 15) E(IC(1), IC(m - 14));
    if constexpr (m == 20) E(IC(1), IC(2));
    if constexpr (m >= 24 && m < 29) E(IC(1), IC(m - 21));
    if constexpr (m >= 29 && m < 37) E(IC(2), IC(m - 29));
    if constexpr (m == 8 || m == 10 || m == 12 || m == 14) C(IC(0), IC((m - 8) / 2));
    if constexpr (m == 13) T1(IC(0));
    if constexpr (m == 15) T2(IC(0));
    if constexpr (m == 18) BR(IC(0));
    if constexpr (m == 17) C(IC(1), IC(0));
    if constexpr (m == 25 || m == 27 || m == 29) C(IC(1), IC((m - 23) / 2));
    if constexpr (m == 28) T1(IC(1));
    if constexpr (m == 30) T2(IC(1));
    if constexpr (m == 32) BR(IC(1));
    if constexpr (m == 31 || m == 33 || m == 35 || m == 37) C(IC(2), IC((m - 31) / 2));
    if constexpr (m == 37) T1(IC(2));
    if constexpr (m == 38) T2(IC(2));
    if constexpr (m == 41) BR(IC(2));
    SBAR();
  };
  for_seq(gap, std::make_integer_sequence<int, 51>{});
}

DI void att_init(Att& A, f32x4 (&acc)[8][3], bool odd) {
#pragma unroll
  for (int a = 0; a < 4; ++a)
#pragma unroll
    for (int q = 0; q < 3; ++q) {
      A.q[a][q] = __builtin_bit_cast(bf16x8, pack_pair(acc[2 * a][q], acc[2 * a + 1][q], QSCALE, odd));
      A.o[2 * a][q] = f32x4{0.f, 0.f, 0.f, 0.f}; A.o[2 * a + 1][q] = f32x4{0.f, 0.f, 0.f, 0.f};
    }
#pragma unroll
  for (int q = 0; q < 3; ++q) { A.l[q] = f32x4{0.f, 0.f, 0.f, 0.f}; A.nm[q] = f32x4{0.f, 0.f, 0.f, 0.f}; }
  A.ones = u32x4{0x3f803f80u, 0x3f803f80u, 0x3f803f80u, 0x3f803f80u}; asm volatile("" : "+v"(A.ones));
}
DI void att_read_k(Att& A, unsigned ka) {
#pragma unroll
  for (int i = 0; i < 8; ++i) A.kf[i] = ld128(ka + (i >> 2) * 4096 + (i & 3) * 256);
  SBAR(); wait_lgkm<0>(); SBAR();
}
DI void att_store_o(Att& A, unsigned obuf_lane, bool odd) {
#pragma unroll
  for (int q = 0; q < 3; ++q) {
    const float inv = __builtin_amdgcn_rcpf(A.l[q][0]);
#pragma unroll
    for (int a = 0; a < 4; ++a) *(LDS_AS u32x4*)(obuf_lane + q * 4096 + a * 1024) = pack_pair(A.o[2 * a][q], A.o[2 * a + 1][q], inv, odd);
  }
  wait_lgkm0();
}
DI void store_part(f32x4 (&acc)[8][3], char* dst, unsigned lane) {
  const __amdgpu_buffer_rsrc_t r = __builtin_amdgcn_make_buffer_rsrc(dst, 0, 12288, 0x00020000);
  const bool odd = (lane >> 4) & 1;
#pragma unroll
  for (int a = 0; a < 4; ++a)
#pragma unroll
    for (int q = 0; q < 3; ++q) __builtin_amdgcn_raw_buffer_store_b128(pack_pair(acc[2 * a][q], acc[2 * a + 1][q], 1.0f, odd), r, lane * 16 + (a * 3 + q) * 1024, 0, K_STAUX);
}
typedef __attribute__((address_space(1))) u64 gu64;
DI void publish(u64* fl  , unsigned j, unsigned w, u64 tok, unsigned lane) {
  wait_vm<0>();
  if (lane < 6) __hip_atomic_store((gu64*)(fl + ((2 * w + lane / 3) * 3 + lane % 3) * 8 + j), tok, __ATOMIC_RELAXED, __HIP_MEMORY_SCOPE_AGENT);
}
DI void reduce_out(const char* part  , u64* fl  , char* out  , unsigned j, unsigned q, u64 tok, unsigned lane) {
  const u32x4 srd = make_srd(part + (j >> 1) * 12288 + ((j & 1) * 6 + q) * 1024, 7 * 49152 + 4096);
  gu64* f = (gu64*)(fl + (j * 3 + q) * 8 + (lane & 7));
  const unsigned vo = lane * 16;
  u32x4 v[8][2];
#pragma unroll
  for (int i = 0; i < 8; ++i) { v[i][0] = u32x4{0, 0, 0, 0}; v[i][1] = u32x4{0, 0, 0, 0}; }
  unsigned done = 0;
#pragma unroll 1
  for (unsigned spin = 0; spin < (1u << 16); ++spin) {
    const u64 x = __hip_atomic_load(f, __ATOMIC_RELAXED, __HIP_MEMORY_SCOPE_AGENT);
    const unsigned rdy = (unsigned)__ballot(x == tok) & 255u, nw = rdy & ~done;
#pragma unroll
    for (int i = 0; i < 8; ++i)
      if (nw & (1u << i))
        { if (K_PNT) asm volatile("buffer_load_dwordx4 %0, %2, %3, %4 offen sc1 nt\n\tbuffer_load_dwordx4 %1, %2, %3, %4 offen offset:3072 sc1 nt"
                     : "+v"(v[i][0]), "+v"(v[i][1]) : "v"(vo), "s"(srd), "s"(i * 49152u) : "memory");
          else asm volatile("buffer_load_dwordx4 %0, %2, %3, %4 offen sc1\n\tbuffer_load_dwordx4 %1, %2, %3, %4 offen offset:3072 sc1"
                     : "+v"(v[i][0]), "+v"(v[i][1]) : "v"(vo), "s"(srd), "s"(i * 49152u) : "memory"); }
    done |= nw;
    if (done == 255u) break;
    __builtin_amdgcn_s_sleep(2);
  }
  asm volatile("s_waitcnt vmcnt(0)" : "+v"(v[0][0]), "+v"(v[0][1]), "+v"(v[1][0]), "+v"(v[1][1]), "+v"(v[2][0]), "+v"(v[2][1]), "+v"(v[3][0]), "+v"(v[3][1]),
               "+v"(v[4][0]), "+v"(v[4][1]), "+v"(v[5][0]), "+v"(v[5][1]), "+v"(v[6][0]), "+v"(v[6][1]), "+v"(v[7][0]), "+v"(v[7][1]) :: "memory");
  char* o = out + (size_t)(q * 16 + (lane & 15)) * 2048 + j * 128 + (lane >> 4) * 16;
#pragma unroll
  for (int a = 0; a < 2; ++a) {
    float s[8] = {0, 0, 0, 0, 0, 0, 0, 0};
#pragma unroll
    for (int i = 0; i < 8; ++i)
#pragma unroll
      for (int e = 0; e < 4; ++e) { s[2 * e] += __uint_as_float(v[i][a][e] << 16); s[2 * e + 1] += __uint_as_float(v[i][a][e] & 0xffff0000u); }
    *(u32x4*)(o + a * 64) = u32x4{cvtpk(s[0], s[1]), cvtpk(s[2], s[3]), cvtpk(s[4], s[5]), cvtpk(s[6], s[7])};
  }
  if (lane < 8) __hip_atomic_store(f, ~tok, __ATOMIC_RELAXED, __HIP_MEMORY_SCOPE_AGENT);
}
DI u64 get_token() {
  lds_wait_ge(W_TOKV, 1);
  const unsigned lo = lds_peek(W_TOK), hi = lds_peek(W_TOK + 4);
  return ((u64)__builtin_amdgcn_readfirstlane(hi) << 32) | __builtin_amdgcn_readfirstlane(lo);
}
template <int NR, int MODE> DI void touch_rows(const char* p0, unsigned stride, u32x4& t) {
#pragma unroll
  for (int i = 0; i < NR; ++i) {
    if (MODE == 1) asm volatile("global_load_dwordx4 %0, %1, off nt" : "+v"(t) : "v"(p0 + (size_t)i * stride) : "memory");
    else if (MODE == 2) asm volatile("global_load_dwordx4 %0, %1, off sc1" : "+v"(t) : "v"(p0 + (size_t)i * stride) : "memory");
    else asm volatile("global_load_dwordx4 %0, %1, off" : "+v"(t) : "v"(p0 + (size_t)i * stride) : "memory");
  }
}
template <int NG, class P, class S> DI void warm(P ptr, S stride, bool go) {
  u32x4 t = {0, 0, 0, 0};
  if (K_HOM) {
#pragma unroll
    for (int g = 0; g < NG; ++g)
#pragma unroll
      for (int i = 0; i < 4; ++i) { touch_rows<1, 1>(ptr(g) + (size_t)i * stride(g), 0, t); touch_rows<1, K_HOM == 1 ? 2 : 0>(ptr(g) + (size_t)i * stride(g), 0, t); }
    if (go) lds_add(W_GO, 1);
  } else
#pragma unroll
  for (int g = 0; g <= NG; ++g) {
    if (g < NG) touch_rows<4, 1>(ptr(g), stride(g), t);
    if (g >= 1) {
      if (g == 1 || g == NG) wait_vm<4>(); else wait_vm<8>();
      if (g == NG && go) lds_add(W_GO, 1);
      touch_rows<4, 2>(ptr(g - 1), stride(g - 1), t);
    }
  }
  asm volatile("s_waitcnt vmcnt(0)" : "+v"(t) :: "memory");
}
template <int NT> DI void load_x(const char* x, unsigned w, unsigned r, unsigned lane) {
  const u32x4 sx = make_srd(x, 49152);
  const unsigned v = src_lane<10>(lane);
  const unsigned v0 = v ^ (((3 * w) & 2) << 4), v1 = (v ^ (((3 * w + 1) & 2) << 4)) + 3072, v2 = (v ^ (((3 * w + 2) & 2) << 4)) + 6144;
#pragma unroll
  for (int i = 0; i < 4; ++i) { const unsigned kq = i ^ r, so = kq * 256 + w * 12288;
    set_m0(RX + kq * 12288 + w * 3072); dma<0, NT>(sx, v0, so); dma<1024, NT>(sx, v1, so); dma<2048, NT>(sx, v2, so); }
}

__global__ __launch_bounds__(512, 2) void fused_k(Params p) {
  __shared__ __attribute__((aligned(1024))) char smem[LDS_BYTES];
  asm volatile("" :: "v"((LDS_AS char*)smem));
  const unsigned tid = threadIdx.x;
  unsigned lane;
#define NEWLANE() (lane = fresh_lane())
#define ODD ((bool)((lane >> 4) & 1))
  const unsigned wid = __builtin_amdgcn_readfirstlane(tid >> 6), w = wid & 3;
  const bool isB = wid < 4;
  const unsigned j = blockIdx.x & 7, b = blockIdx.x >> 3, h = 4 * j + w;
  const bool late = K_G2 && (b & 1);
  const unsigned rot = K_G2 ? (b >> 1) : (b & 15), rank = K_G2 ? (b >> 1) + 16 * (b & 1) : b;
  const unsigned t0 = now_ticks();
  asm volatile("" :: "s"(p.x[0]), "s"(p.x[1]), "s"(p.k[0]), "s"(p.k[1]), "s"(p.v[0]), "s"(p.v[1]), "s"(p.wq[0]), "s"(p.wq[1]), "s"(p.wo[0]), "s"(p.wo[1]), "s"(p.part), "s"(p.flags), "s"(p.out), "s"(t0));
  STAMP(0);
  if (tid < 64) *(LDS_AS unsigned*)(RS + tid * 4) = 0;
  wait_lgkm0();
  __builtin_amdgcn_s_barrier();
  f32x4 acc[8][3];
  Att A;
  GemmCfg G;
  const float ninf = -__builtin_inff();
  u64* const epoch = p.flags + 2 * 32 * 192 + b * 16;
  if (isB) {
    const unsigned ring = RB + w * 16384;
    NEWLANE();
    const u32x4 sk = make_srd(p.k[1] + (size_t)(b * 32 + h) * 131072, 131072), sv = make_srd(p.v[1] + (size_t)(b * 32 + h) * 131072, 131072);
    const unsigned s0 = src_lane<8>(lane), s16 = s0 ^ 16u, s32 = s0 ^ 32u;
    auto kv0 = [&](int i) {
      const unsigned so = ((i >> 1) & 1) * 4096;
      if ((i & 1) == 0) { set_m0(ring + (i >> 1) * 4096); if (i < 4) { dma<0>(sk, s0, so); dma<1024>(sk, s0, so); } else { dma<0>(sv, s0, so); dma<1024>(sv, s16, so); } }
      else { if (i < 4) { dma<2048>(sk, s32, so); dma<3072>(sk, s32, so); } else { dma<2048>(sv, s0, so); dma<3072>(sv, s16, so); } } };
    gemm_cfg<13>(G, p.wq[1] + h * 256, RA + w * 8192, rd_rows(lane) + RX, rot, W_XB, lane);
    if (late) {
      wait_until(t0, K_TG2);
#pragma unroll
      for (int i = 0; i < 8; ++i) kv0(i);
      lds_wait_ge(W_GO, 4);
    }
    gemm_prime<13, 2>(G);
    STAMP(1);
    gemm<13, 2, 256, 12288, 1, K_QBNT>(acc, G, [&](int i) { if (!late && (i & 1) == 0) kv0(i >> 1); }, [&] { STAMP(5); });
    lds_add(W_BQP, 1);
    STAMP(2);
    NEWLANE();
    const unsigned ka = rd_rows(lane) + ring, va0 = rd_cols(lane) + ring + 8192, va1 = va0 ^ 8u;
    att_init(A, acc, ODD);
    if (!late) { kv0(4); kv0(5); kv0(6); kv0(7); }
    wait_vm<8>();
    att_read_k(A, ka);
    float floorv = ninf; unsigned force = 0;
    const unsigned pmask = 0xff00u | (1u << w);
    unsigned pz = 0;
#pragma unroll 1
    for (unsigned c = 0; c < 16; ++c) {
      const unsigned so = c * 8192 + 8192, so2 = so + 4096; unsigned ps = 0;
      const bool more = c != 15;
      att_chunk(A, floorv, force, va0, va1, ka, [&](auto mc) {
        constexpr int m = decltype(mc)::value;
        if constexpr (m == 42 || m == 15) pz = lds_peek(W_PAUSE);
        if constexpr (m == 1 || m == 26) ps = to_sgpr(pz);
        if constexpr (m == 4 || m == 38) { STAMP((m == 4 ? 16 : 32) + c);
          if (more) { if (K_TS && c >= K_PC0) wait_until(t0, K_TS - K_PD * (30 - 2 * c - (m == 4 ? 0 : 1)));
            if (__builtin_expect(ps & pmask, 0)) lds_wait_clear(W_PAUSE, pmask);
            if (K_BD < 12) wait_vm<K_BD - 4>();
            if (m == 4) dma4(ring, sk, s0, s0, s32, s32, so); else dma4(ring + 8192, sv, s0, s16, s0, s16, so); } }
        if constexpr (m == 5 || m == 40) { if (more) { if (K_BD < 16) wait_vm<K_BD - 4>();
            if (m == 5) dma4(ring + 4096, sk, s0, s0, s32, s32, so2); else dma4(ring + 12288, sv, s0, s16, s0, s16, so2); } }
        if constexpr (m == 5 && K_CA + K_CB > 0) { if (__builtin_expect(c == K_CA || c == K_CB, 0)) {
            const u32x4 sw = make_srd(p.wo[c == K_CA ? 0 : 1] + (size_t)(512 * j + 16 * b + 4 * w) * 1024, 4096);
            set_m0(RDUMMY);
            dma<0, 1>(sw, s0, 0); dma<0, 2>(sw, s0, 0); dma<1024, 1>(sw, s0, 0); dma<1024, 2>(sw, s0, 0);
            dma<2048, 1>(sw, s0, 0); dma<2048, 2>(sw, s0, 0); dma<3072, 1>(sw, s0, 0); dma<3072, 2>(sw, s0, 0); } }
        if constexpr (m == 16) { wait_vm<8>(); if (!more) wait_vm<0>(); }
        if constexpr (m == 43) wait_vm<8>();
      });
      floorv = 0.f; force = 1;
    }
    STAMP(3);
    wait_lgkm0();
    NEWLANE();
    att_store_o(A, ring + lane * 16, ODD);
    lds_add(W_OB + 4 * w, 1);
    STAMP(4);
    lds_add(W_BDONE, 1);
    const u64 tok = get_token();
    lds_wait_ge(W_PARK + 4 * w, 1);
    if (K_TW) wait_until(t0, K_TW);
    if (K_PW == 5) lds_wait_ge(W_WDONE, 4); else lds_wait_ge(W_BDONE, K_PW);
    { const __amdgpu_buffer_rsrc_t r = __builtin_amdgcn_make_buffer_rsrc(p.part + (size_t)((b * 8 + j) * 4 + w) * 12288, 0, 12288, 0x00020000);
#pragma unroll
      for (int f = 0; f < 12; ++f) __builtin_amdgcn_raw_buffer_store_b128(*(LDS_AS u32x4*)(RX + w * 12288 + f * 1024 + lane * 16), r, lane * 16 + f * 1024, 0, K_STAUX); }
    publish(p.flags + b * 192, j, w, tok, lane);
    STAMP(7);
    if (w < 3) {
      lds_wait_ge(W_WDONE, 4);
      reduce_out(p.part + (size_t)(b * 8) * 49152, p.flags + b * 192, p.out + (size_t)b * 98304, j, w, tok, lane);
      STAMP(8);
      if (j == 0 && w == 0 && lane == 0) __hip_atomic_store((gu64*)epoch, tok, __ATOMIC_RELAXED, __HIP_MEMORY_SCOPE_AGENT);
    }
    STAMP(6);
    STAMP_DUMP();
  } else {
    unsigned tlb = 0;
    NEWLANE();
    if (K_TLB) {
      const unsigned l = lane & 7;
      const char* q = l == 0 ? p.k[1] + (size_t)(b * 32 + h) * 131072 : l == 1 ? p.v[1] + (size_t)(b * 32 + h) * 131072 : l == 2 ? p.k[0] + (size_t)(b * 8 + j) * 131072
                    : l == 3 ? p.v[0] + (size_t)(b * 8 + j) * 131072 : l == 4 ? p.x[0] + (size_t)b * 49152 : l == 5 ? p.wq[0] + (size_t)w * 1048576 + j * 1024
                    : l == 6 ? p.wo[0] + (size_t)j * 524288 : p.wo[1] + (size_t)j * 524288;
      unsigned t; asm volatile("global_load_dword %0, %1, off nt" : "=v"(t) : "v"(q) : "memory");
      if (late) asm volatile("s_waitcnt vmcnt(0)" : "+v"(t) :: "memory");
      tlb = t;
    }
    if (late) wait_until(t0, K_TG2);
    { const unsigned r = rot >> 2;
      load_x<K_XB>(p.x[1] + (size_t)b * 49152, w, r, lane);
      wait_vm<9>(); asm volatile("" :: "v"(tlb)); lds_add(W_XB + 4 * r, 1);
      wait_vm<6>(); lds_add(W_XB + 4 * (r ^ 1), 1);
      wait_vm<3>(); lds_add(W_XB + 4 * (r ^ 2), 1);
      wait_vm<0>(); lds_add(W_XB + 4 * (r ^ 3), 1); }
    if (APRIO) __builtin_amdgcn_s_setprio(APRIO);
    STAMP(1);
    const unsigned r0 = ((b >> 1) * 4 + w) * 8;
    if (late) {
      const char* s0 = p.wq[0] + (size_t)r0 * 8192 + j * 1024 + lane * 16;
      const char* s1 = p.wo[0] + (size_t)(512 * j + r0) * 1024 + lane * 16;
      const char* s2 = p.wo[1] + (size_t)(512 * j + r0) * 1024 + lane * 16;
      warm<2 * K_WL>([&](int g) { return g < 2 ? s0 + (g & 1) * 32768 : (g < 4 ? s1 : s2) + (g & 1) * 4096; }, [&](int g) { return g < 2 ? 8192u : 1024u; }, true);
    }
    STAMP(13);
    lds_wait_ge(W_BQP, 4);
    load_x<(K_XA > 0)>(p.x[0] + (size_t)b * 49152, w, 0, lane);
    if (w == 0) {
      const u64 tk = __hip_atomic_load((gu64*)epoch, __ATOMIC_RELAXED, __HIP_MEMORY_SCOPE_AGENT) + 0x9E3779B97F4A7C15ull;
      if (lane == 0) *(LDS_AS u64*)W_TOK = tk;
      wait_lgkm0();
      lds_add(W_TOKV, 1);
    }
    if (K_WL < 3 && !late && K_CA + K_CB == 0) {
      const char* s1 = p.wo[0] + (size_t)(512 * j + r0) * 1024 + lane * 16;
      const char* s2 = p.wo[1] + (size_t)(512 * j + r0) * 1024 + lane * 16;
      warm<6 - 2 * K_WL>([&](int g) { return (g < 2 ? s2 : s1) + (g & 1) * 4096; }, [&](int) { return 1024u; }, false);
    }
    wait_vm<0>();
    unsigned bar = 4;
    lds_add(W_ABAR, 1);
    STAMP(14);
    wait_until(t0, K_TQ0 + K_DQ * rank);
    lds_wait_ge(W_ABAR, bar);
    STAMP(2);
    const unsigned ring = RA + w * 8192, nhold = (K_TN0 * (31 - rank) + K_TN1 * rank + 15) / 31;
    lds_add(W_PAUSE, 256u);
    NEWLANE();
    gemm_cfg<13>(G, p.wq[0] + h * 256, ring, rd_rows(lane) + RX, rot, 0, lane);
    gemm_prime<13, 2>(G);
    gemm<13, 2, 256, 12288, 0, K_WNT>(acc, G, [&](int i) { if (i == 0) lds_add(W_PAUSE, w == 0 && nhold ? K_HM - 256u : 0u - 256u); }, [&] { STAMP(9); });
    lds_add(W_ABAR, 1); bar += 4;
    STAMP(3);
    NEWLANE();
    u32x4 skv = make_srd((w < 2 ? p.k[0] : p.v[0]) + (size_t)(b * 8 + j) * 131072 + (w & 1) * 4096, 131072 - 4096);
    const unsigned s0 = src_lane<8>(lane), s1 = s0 ^ (w < 2 ? 0u : 16u), s2 = s0 ^ (w < 2 ? 32u : 0u), s3 = s0 ^ (w < 2 ? 32u : 16u);
    lds_wait_ge(W_ABAR, bar);
#pragma unroll
    for (int c = 0; c < NSA - 1; ++c) dma4(RX + c * 16384 + w * 4096, skv, s0, s1, s2, s3, c * 8192);
    unsigned so = (NSA - 1) * 8192;
    set_m0(RX + (NSA - 1) * 16384 + w * 4096); dma<0>(skv, s0, so); dma<1024>(skv, s1, so);
    att_init(A, acc, ODD);
    wait_vm<4 * (NSA - 1) - 2>();
    lds_add(W_ABAR, 1); bar += 4;
    lds_wait_ge(W_ABAR, bar);
    const unsigned kl = rd_rows(lane) + RX, vl = rd_cols(lane) + RX + 8192;
    att_read_k(A, kl);
    float floorv = ninf; unsigned force = 0;
    unsigned cur = 0;
#pragma unroll 1
    for (unsigned c = 0; c < 16; ++c) {
      const unsigned nxt = cur == (NSA - 1) * 16384 ? 0u : cur + 16384;
      const unsigned va0 = vl + cur, va1 = va0 ^ 8u, ka = kl + nxt;
      unsigned bz = 0, bs = 0;
      bar += 4;
      att_chunk(A, floorv, force, va0, va1, ka, [&](auto mc) {
        constexpr int m = decltype(mc)::value;
        if constexpr (m == K_P2) dma<2048>(skv, s2, so);
        if constexpr (m == K_P3) dma<3072>(skv, s3, so);
        if constexpr (m == 2) { if (w == 0 && c + 1 == nhold) lds_add(W_PAUSE, 0u - K_HM); }
        if constexpr (m == K_ARR) { wait_vm<4 * (NSA - 2)>(); STAMP(16 + c); lds_add(W_ABAR, 1); }
        if constexpr (m == 37) bz = lds_peek(W_ABAR);
        if constexpr (m == 39) { so += 8192; if (c + NSA == 16) skv[2] = 0;
                                 set_m0(c + NSA < 16 ? RX + cur + w * 4096 : RDUMMY); }
        if constexpr (m == 40) bs = to_sgpr(bz);
        if constexpr (m == 42) { if (__builtin_expect(bs < bar, 0)) lds_wait_ge(W_ABAR, bar); }
        if constexpr (m == K_P0) dma<0>(skv, s0, so);
        if constexpr (m == K_P1) dma<1024>(skv, s1, so);
      });
      floorv = 0.f; force = 1; cur = nxt;
    }
    STAMP(4);
    wait_lgkm0();
    NEWLANE();
    gemm_cfg<10>(G, p.wo[0] + (size_t)j * 524288 + w * 256, ring, RX + lane * 16, K_OROT ? (b & 15) : 0, 0, lane);
    wait_until(t0, K_TO0 + K_DO * rank);
    STAMP(12);
    lds_add(W_PAUSE, 256u);
    att_store_o(A, RX + w * 12288 + lane * 16, ODD);
    lds_add(W_ABAR, 1); bar += 4;
    gemm_prime<10, 2>(G);
    lds_wait_ge(W_ABAR, bar);
    const u32x4 swb = make_srd(p.wo[1] + (size_t)j * 524288 + w * 256, (511u << 10) + 256u);
    gemm<10, 2, 1024, 12288, 0, K_WNT>(acc, G, [&](int i) {
      if (i == 0 || i == 4) set_m0(ring + (i >> 2) * 4096);
      const unsigned so = w_off<10>(0, i >> 2, K_OROT ? (b & 3) : 0);
      if ((i & 3) == 0) dma<0>(swb, G.wv[0], so); else if ((i & 3) == 1) dma<1024>(swb, G.wv[1], so); else if ((i & 3) == 2) dma<2048>(swb, G.wv[2], so); else dma<3072>(swb, G.wv[3], so);
      if (i == 7) lds_add(W_PAUSE, 0u - 256u); }, [&] { STAMP(10); });
    lds_add(W_ABAR, 1); bar += 4;
    STAMP(5);
    NEWLANE();
    lds_wait_ge(W_ABAR, bar);
#pragma unroll
    for (int a = 0; a < 4; ++a)
#pragma unroll
      for (int q = 0; q < 3; ++q) *(LDS_AS u32x4*)(RX + w * 12288 + (a * 3 + q) * 1024 + lane * 16) = pack_pair(acc[2 * a][q], acc[2 * a + 1][q], 1.0f, ODD);
    wait_lgkm0();
    lds_add(W_PARK + 4 * w, 1);
    gemm_cfg<10>(G, p.wo[1] + (size_t)j * 524288 + w * 256, ring, RB + lane * 16, K_OROT ? (b & 3) : 0, W_OB, lane);
    const u64 tok = get_token();
    STAMP(6);
    gemm<10, 2, 1024, 16384, 1 + K_OBP, K_WNT>(acc, G, [](int i) { if (i == 0) lds_add(W_WDONE, 1); }, [&] { STAMP(11); });
    STAMP(7);
    NEWLANE();
    store_part(acc, p.part + (size_t)(((32 + b) * 8 + j) * 4 + w) * 12288, lane);
    publish(p.flags + (32 + b) * 192, j, w, tok, lane);
    STAMP(15);
    if (w < 3) reduce_out(p.part + (size_t)(256 + b * 8) * 49152, p.flags + (32 + b) * 192, p.out + (size_t)b * 98304 + 1024, j, w, tok, lane);
    STAMP(8);
    STAMP_DUMP();
  }
}

extern "C" void kernel_launch(void* const* d_in, const int* in_sizes, int n_in, void* d_out, int out_size, void* d_ws, size_t ws_size, hipStream_t stream) {
  Params p;
  for (int br = 0; br < 2; ++br) {
    p.x[br] = (const char*)d_in[br * 5 + 0]; p.k[br] = (const char*)d_in[br * 5 + 1]; p.v[br] = (const char*)d_in[br * 5 + 2];
    p.wq[br] = (const char*)d_in[br * 5 + 3]; p.wo[br] = (const char*)d_in[br * 5 + 4];
  }
  p.part = (char*)d_ws; p.flags = (u64*)((char*)d_ws + (32u << 20)); p.out = (char*)d_out; p.stamps = (u64*)((char*)d_ws + (64u << 20));
  fused_k<<<256, 512, 0, stream>>>(p);
}
```

```cpp
#include <hip/hip_runtime.h>
#include <stdint.h>
#include <utility>

typedef __bf16 bf16x8 __attribute__((ext_vector_type(8)));
typedef __bf16 bf16x4 __attribute__((ext_vector_type(4)));
typedef __bf16 bf16x2 __attribute__((ext_vector_type(2)));
typedef float f32x4 __attribute__((ext_vector_type(4)));
typedef float f32x2 __attribute__((ext_vector_type(2)));
typedef unsigned u32x4 __attribute__((ext_vector_type(4)));
typedef unsigned long long u64;
typedef __attribute__((address_space(1))) u64 gu64;
#define LDS_AS __attribute__((address_space(3)))
#define LDS_PTR(T, a) ((LDS_AS T*)(uintptr_t)(a))
#define DI __device__ __forceinline__
#define INL __attribute__((always_inline))
#define SBAR() __builtin_amdgcn_sched_barrier(0)

constexpr unsigned T_LATE = 200;
constexpr unsigned T_QA = 1050, T_STEP = 67;
constexpr unsigned TURNS0 = 0x8f4f2f1fu, TURNS1 = 0x8d4e271bu, TURNS2 = 0x894c2613u;

constexpr unsigned RB = 0;
constexpr unsigned RX = 65536;
constexpr unsigned RA = 114688;
constexpr unsigned ROWN = RX + 32768;
constexpr unsigned RDUMMY = 155648;
constexpr unsigned RS = 163584, LDS_BYTES = 163840;
constexpr unsigned W_ABAR = RS, W_GO = RS + 16, W_BQP = RS + 32, W_PAUSE = RS + 48, W_OB = RS + 64, W_XB = RS + 96, W_TOKV = RS + 112, W_TOK = RS + 128  ,
                   W_SENT = RS + 144, W_AFULL = RS + 160, W_WDONE = RS + 176, W_AFREE = RS + 192, W_RED = RS + 208;
constexpr unsigned NSA = 4;
constexpr float QSCALE = 0.08838834764831845f * 1.4426950408889634f;
constexpr float BIAS = 7.0f;

struct Params {
  const char* x[2]; const char* k[2]; const char* v[2]; const char* wq[2]; const char* wo[2];
  char* part; u64* flags; char* out;
};

DI u32x4 make_srd(const char* p, unsigned bytes) { u32x4 s = {(unsigned)(uintptr_t)p, (unsigned)((uintptr_t)p >> 32), bytes, 0x00020000u}; return s; }
template <int N> DI void wait_vm() { asm volatile("s_waitcnt vmcnt(%0)" :: "i"(N) : "memory"); }
DI void wait_lgkm0() { asm volatile("s_waitcnt lgkmcnt(0)" ::: "memory"); }
template <int N> DI void wait_lgkm() { __builtin_amdgcn_s_waitcnt(0xC07F | (N << 8)); }
DI void set_m0(unsigned dst) { asm volatile("s_mov_b32 m0, %0\n\ts_nop 0" :: "s"(dst) : "memory"); }
template <int OFF, int NT> DI void dma(u32x4 srd, unsigned v, unsigned so) {
  if constexpr (NT) asm volatile("buffer_load_dwordx4 %0, %1, %3 offen offset:%2 nt lds" :: "v"(v), "s"(srd), "i"(OFF), "s"(so) : "memory");
  else asm volatile("buffer_load_dwordx4 %0, %1, %3 offen offset:%2 lds" :: "v"(v), "s"(srd), "i"(OFF), "s"(so) : "memory");
}
DI void dma4(unsigned dst, u32x4 srd, unsigned v0, unsigned v1, unsigned v2, unsigned v3, unsigned so) {
  set_m0(dst); dma<0, 1>(srd, v0, so); dma<1024, 1>(srd, v1, so); dma<2048, 1>(srd, v2, so); dma<3072, 1>(srd, v3, so);
}
DI bf16x4 tr_read(unsigned a) { return __builtin_amdgcn_ds_read_tr16_b64_v4bf16(LDS_PTR(bf16x4, a)); }
DI bf16x8 ld128(unsigned a) { return *LDS_PTR(bf16x8, a); }
DI bf16x8 cat(bf16x4 a, bf16x4 b) { return __builtin_shufflevector(a, b, 0, 1, 2, 3, 4, 5, 6, 7); }
DI f32x4 mfma(bf16x8 a, bf16x8 b, f32x4 c) { return __builtin_amdgcn_mfma_f32_16x16x32_bf16(a, b, c, 0, 0, 0); }
DI unsigned cvtpk(float lo, float hi) { f32x2 v = {lo, hi}; bf16x2 b = __builtin_convertvector(v, bf16x2); return __builtin_bit_cast(unsigned, b); }
DI u32x4 pack_raw(f32x4 t0, f32x4 t1) { return u32x4{cvtpk(t0[0], t0[1]), cvtpk(t0[2], t0[3]), cvtpk(t1[0], t1[1]), cvtpk(t1[2], t1[3])}; }
DI u32x4 pack_pair(f32x4 t0, f32x4 t1, bool odd) { const u32x4 r = pack_raw(t0, t1); return u32x4{odd ? r[2] : r[0], odd ? r[3] : r[1], odd ? r[0] : r[2], odd ? r[1] : r[3]}; }
DI unsigned fresh_lane() { unsigned l; asm volatile("v_mbcnt_lo_u32_b32 %0, -1, 0\n\tv_mbcnt_hi_u32_b32 %0, -1, %0" : "=v"(l)); return l; }
DI float xmax16_32(float d) {
  auto r = __builtin_amdgcn_permlane16_swap(__float_as_uint(d), __float_as_uint(d), false, false);
  d = fmaxf(__uint_as_float(r[0]), __uint_as_float(r[1]));
  auto u = __builtin_amdgcn_permlane32_swap(__float_as_uint(d), __float_as_uint(d), false, false);
  return fmaxf(__uint_as_float(u[0]), __uint_as_float(u[1]));
}

DI void lds_add(unsigned addr, unsigned val) {
  asm volatile("s_mov_b64 exec, 1\n\tds_add_u32 %0, %1\n\ts_mov_b64 exec, -1" :: "v"(addr), "v"(val) : "memory");
}
DI unsigned lds_add_rtn(unsigned addr, unsigned val) {
  unsigned v, r;
  asm volatile("s_mov_b64 exec, 1\n\tds_add_rtn_u32 %0, %2, %3\n\ts_waitcnt lgkmcnt(0)\n\tv_readfirstlane_b32 %1, %0\n\ts_mov_b64 exec, -1\n\ts_nop 0\n\tv_mov_b32 %0, %1\n\ts_nop 0"
               : "=&v"(v), "=&s"(r) : "v"(addr), "v"(val) : "memory");
  return v;
}
#define LDS_SPIN(TEST, SLEEP) "1:\n\tds_read_b32 %0, %3\n\ts_waitcnt lgkmcnt(0)\n\tv_readfirstlane_b32 %1, %0\n\ts_nop 0\n\t" TEST "s_sleep " SLEEP "\n\ts_sub_u32 %2, %2, 1\n\ts_cbranch_scc0 1b\n2:"
DI void lds_wait_ge(unsigned addr, unsigned target) {
  unsigned v, sv, cnt = 1u << 18;
  asm volatile(LDS_SPIN("s_cmp_ge_u32 %1, %4\n\ts_cbranch_scc1 2f\n\t", "1") : "=&v"(v), "=&s"(sv), "+&s"(cnt) : "v"(addr), "s"(target) : "memory", "scc");
}
DI void lds_wait_all_ge(unsigned addr, unsigned n) {
  unsigned v, sv, cnt = 1u << 18;
  asm volatile(LDS_SPIN("s_add_u32 %1, %1, %4\n\ts_andn2_b32 %1, 0x80808080, %1\n\ts_cbranch_scc0 2f\n\t", "1")
               : "=&v"(v), "=&s"(sv), "+&s"(cnt) : "v"(addr), "s"((128u - n) * 0x01010101u) : "memory", "scc");
}
DI void lds_wait_set(unsigned addr, unsigned set) {
  unsigned v, sv, cnt = 1u << 18;
  asm volatile(LDS_SPIN("s_and_b32 %1, %1, %4\n\ts_cbranch_scc1 2f\n\t", "2") : "=&v"(v), "=&s"(sv), "+&s"(cnt) : "v"(addr), "s"(set) : "memory", "scc");
}
DI unsigned lds_wait_clear_or(unsigned addr, unsigned mask, unsigned set) {
  unsigned v, sv, cnt = 1u << 18;
  asm volatile(LDS_SPIN("s_and_b32 %1, %1, %4\n\ts_cbranch_scc0 2f\n\ts_and_b32 %1, %1, %5\n\ts_cbranch_scc1 2f\n\t", "2")
               : "=&v"(v), "=&s"(sv), "+&s"(cnt) : "v"(addr), "s"(mask), "s"(set) : "memory", "scc");
  return __builtin_amdgcn_readfirstlane(v);
}
DI unsigned lds_peek(unsigned addr) { return *(volatile LDS_AS unsigned*)(uintptr_t)addr; }
DI unsigned to_sgpr(unsigned v) { unsigned r = __builtin_amdgcn_readfirstlane(v); asm volatile("" : "+s"(r)); return r; }
DI unsigned now_ticks() { return (unsigned)__builtin_amdgcn_s_memrealtime(); }
DI void wait_until(unsigned t0, unsigned ticks) {
#pragma unroll 1
  while (now_ticks() - t0 < ticks) __builtin_amdgcn_s_sleep(4);
}

template <int SH> DI unsigned src_lane(unsigned lane) { return (((lane >> 2) & 3) << SH) + (lane >> 4) * 64 + (lane & 3) * 16; }
DI unsigned rd_rows(unsigned lane) { const unsigned g = lane >> 4, m = lane & 15; return (m >> 2) * 1024 + (m & 3) * 64 + ((g ^ ((m >> 2) & 2)) << 4); }
DI unsigned rd_cols(unsigned lane) { const unsigned g = lane >> 4, q = (lane >> 2) & 3, pp = lane & 3; return g * 1024 + q * 64 + ((pp ^ (g & 1)) << 4) + 8 * (pp & 1); }

struct Frag { bf16x4 lo[8], hi[8]; bf16x8 b[3]; };
struct GemmCfg { u32x4 srd; unsigned wv[4]; unsigned ring, ga0, ga1, bl, rot, gv; };
template <int SH> DI void gemm_cfg(GemmCfg& G, const char* w, unsigned ring, unsigned bl, unsigned rot, unsigned gate, unsigned lane) {
  constexpr unsigned U = (8u << SH) - 1024u;
  const unsigned w0 = src_lane<SH>(lane);
  G.srd = make_srd(w, (511u << SH) + 256u);
  G.wv[0] = w0; G.wv[1] = (w0 ^ 16u) + U; G.wv[2] = w0 + 2 * U; G.wv[3] = (w0 ^ 16u) + 3 * U;
  G.ring = ring; G.ga0 = rd_cols(lane) + ring; G.ga1 = G.ga0 ^ 8u; G.bl = bl; G.rot = rot; G.gv = gate; asm volatile("" : "+v"(G.gv));
}
template <int SH> DI unsigned w_off(unsigned s, unsigned t, unsigned rot) { return (((s ^ rot) << 5) + 4u * t) << SH; }
template <int SH> DI unsigned w_odd(unsigned so) { return SH == 10 ? so ^ (4u << SH) : so; }
template <int U> DI void w_piece(u32x4 srd, const GemmCfg& G, unsigned so, unsigned so1) { dma<1024 * U, 0>(srd, G.wv[U], (U & 1) ? so1 : so); }
template <int SH> DI void w_step(unsigned dst, u32x4 srd, const GemmCfg& G, unsigned s, unsigned rot) {
#pragma unroll
  for (int t = 0; t < 2; ++t) { const unsigned so = w_off<SH>(s, t, rot), so1 = w_odd<SH>(so);
    set_m0(dst + t * 4096); w_piece<0>(srd, G, so, so1); w_piece<1>(srd, G, so, so1); w_piece<2>(srd, G, so, so1); w_piece<3>(srd, G, so, so1); }
}
template <int SH, int NS, int BK, int MODE, int PAR, class Tail>
DI void gstep(f32x4 (&acc)[8][3], const Frag& c, Frag& n, const GemmCfg& G, unsigned s, Tail tail) {
  constexpr unsigned BS = SH == 13 ? 256 : 1024, SL = NS == 4 && PAR == 0 ? RA - RX : 0;
  u32x4 srd = G.srd;
  if (NS == 4 && MODE == 1 && PAR == 0) srd[2] = s < 14 ? srd[2] : 0u;
#define M(k) do { if (MODE != 0) acc[4 * t + (k) / 3][(k) % 3] = mfma(cat(c.lo[4 * t + (k) / 3], c.hi[4 * t + (k) / 3]), c.b[(k) % 3], acc[4 * t + (k) / 3][(k) % 3]); SBAR(); } while (0)
#define RD(i) d[i] = tr_read((((i) & 1) ? G.ga1 : G.ga0) + SL + t * 4096 + ((i) >> 1) * 256)
#define PC(u) do { if (MODE != 2) w_piece<u>(srd, G, so, so1); else { if (u == 0 && t == 0) wait_vm<4 * NS - 8>(); tail(4 * t + u); } } while (0)
  const unsigned se = s ^ G.rot, gbit = 4u << (8 * (se >> 2));
  unsigned xb = 0, gz = 0, gs = 0;
#pragma unroll
  for (int t = 0; t < 2; ++t) {
    bf16x4* d = t ? n.hi : n.lo;
    if (MODE == 2 && t == 0) tail(-1);
    if (MODE != 2 || t == 0) wait_vm<4 * NS - 4>();
    SBAR();
    M(0); RD(0); RD(1); RD(2); SBAR();
    M(1); RD(3); RD(4); RD(5); SBAR();
    M(2); RD(6); RD(7); SBAR();
    M(3);
    if (t == 0 && PAR == 0) gz = lds_peek(G.gv);
    if (t == 1) { if (PAR == 0) { if (__builtin_expect(!(gs & gbit), 0)) lds_wait_set(G.gv, gbit); } n.b[0] = ld128(xb); n.b[1] = ld128(xb + 4096); }
    SBAR();
    const unsigned so = w_off<SH>((s + NS / 2) & 15u, t, G.rot), so1 = w_odd<SH>(so);
    M(4); if (t == 1) n.b[2] = ld128(xb + 8192); SBAR();
    if (MODE != 2) asm volatile("" :: "s"(so), "s"(so1));
    M(5); SBAR();
    M(6); if (MODE == 0) set_m0(G.ring + SL + t * 4096); if (MODE == 1) asm volatile("s_mov_b32 m0, %0" :: "s"(G.ring + SL + t * 4096) : "memory"); SBAR();
    M(7); asm volatile("" ::: "memory"); wait_lgkm<0>(); SBAR(); PC(0); SBAR();
    M(8); PC(1); if (t == 0 && PAR == 0) gs = to_sgpr(gz); SBAR();
    M(9); PC(2); if (t == 0) { xb = G.bl + ((se & 3) * BS + (se >> 2) * BK); asm volatile("" :: "v"(xb)); } SBAR();
    M(10); PC(3); SBAR();
    M(11);
  }
#undef PC
#undef M
#undef RD
}
template <int SH, int NS, int BK, class Tail>
DI void gemm(f32x4 (&acc)[8][3], const GemmCfg& G, Tail tail) {
#pragma unroll
  for (int a = 0; a < 8; ++a)
#pragma unroll
    for (int q = 0; q < 3; ++q) acc[a][q] = f32x4{0.f, 0.f, 0.f, 0.f};
  Frag X, Y;
  gstep<SH, NS, BK, 0, 0>(acc, Y, X, G, 0, tail);
#pragma unroll 1
  for (unsigned s = 1; s < 15; s += 2) {
    gstep<SH, NS, BK, 1, 1>(acc, X, Y, G, s, tail);
    gstep<SH, NS, BK, 1, 0>(acc, Y, X, G, s + 1, tail);
  }
  gstep<SH, NS, BK, 2, 1>(acc, X, Y, G, 15, tail);
#pragma unroll
  for (int i = 0; i < 8; ++i) {
#pragma unroll
    for (int q = 0; q < 3; ++q) acc[i][q] = mfma(cat(Y.lo[i], Y.hi[i]), Y.b[q], acc[i][q]);
    SBAR(); tail(8 + i); SBAR();
  }
}

template <class F, int... Ms> DI void for_seq(F& f, std::integer_sequence<int, Ms...>) { (f(std::integral_constant<int, Ms>{}), ...); }
struct Att { bf16x8 q[4][3]; f32x4 o[8][3]; f32x4 l[3]; f32x4 nm[3]; bf16x8 kf[8]; u32x4 ones; };

template <class Hook>
DI void att_chunk(Att& A, float floorv, unsigned force, unsigned va0, unsigned va1, unsigned ka, Hook hook) {
  f32x4 s[3][2]; u32x4 pw[3]; bf16x4 vlo[8], vhi[8]; float ex[3][8]; unsigned tt[3]; u64 big[3];
#define IC(n) std::integral_constant<int, (n)>{}
  auto mm = [&](auto mc) {
    constexpr int m = decltype(mc)::value;
    if constexpr (m < 24) { constexpr int q = m >> 3, kt = (m >> 2) & 1, i = m & 3; s[q][kt] = mfma(A.kf[4 * kt + i], A.q[i][q], i == 0 ? A.nm[q] : s[q][kt]); }
    else { constexpr int q = (m - 24) / 9, i = (m - 24) % 9;
      if constexpr (i == 0) A.l[q] = mfma(__builtin_bit_cast(bf16x8, A.ones), __builtin_bit_cast(bf16x8, pw[q]), A.l[q]);
      else A.o[i - 1][q] = mfma(cat(vlo[i - 1], vhi[i - 1]), __builtin_bit_cast(bf16x8, pw[q]), A.o[i - 1][q]); }
    SBAR();
  };
  auto E = [&](auto qc, auto ic) { constexpr int q = decltype(qc)::value, i = decltype(ic)::value;
    ex[q][i] = __builtin_amdgcn_exp2f(s[q][i >> 2][i & 3]); asm volatile("" :: "v"(ex[q][i])); SBAR(); };
  auto C = [&](auto qc, auto ic) { constexpr int q = decltype(qc)::value, i = decltype(ic)::value;
    const unsigned wd = cvtpk(ex[q][2 * i], ex[q][2 * i + 1]); asm volatile("" :: "v"(wd)); pw[q][i] = wd; SBAR(); };
  auto T1 = [&](auto qc) { constexpr int q = decltype(qc)::value; tt[q] = pw[q][0] | pw[q][1] | pw[q][2]; asm volatile("" : "+v"(tt[q])); SBAR(); };
  auto T2 = [&](auto qc) { constexpr int q = decltype(qc)::value;
    big[q] = __ballot(((tt[q] | pw[q][3]) & 0x40004000u) >= force); asm volatile("" : "+s"(big[q])); SBAR(); };
  auto BR = [&](auto qc) { constexpr int q = decltype(qc)::value;
    if (__builtin_expect(big[q] != 0, 0)) {
      float d = fmaxf(fmaxf(fmaxf(s[q][0][0], s[q][0][1]), fmaxf(s[q][0][2], s[q][0][3])), fmaxf(fmaxf(s[q][1][0], s[q][1][1]), fmaxf(s[q][1][2], s[q][1][3])));
      d = fmaxf(xmax16_32(d) + BIAS, floorv);
      const float f = __builtin_amdgcn_exp2f(fminf(-d, 0.f));
#pragma unroll
      for (int r = 0; r < 4; ++r) { A.nm[q][r] -= d; A.l[q][r] *= f;
        pw[q][r] = cvtpk(__builtin_amdgcn_exp2f(s[q][r >> 1][2 * (r & 1)] - d), __builtin_amdgcn_exp2f(s[q][r >> 1][2 * (r & 1) + 1] - d)); }
#pragma unroll
      for (int i = 0; i < 8; ++i) A.o[i][q] *= f;
    }
    SBAR(); };
  auto gap = [&](auto mc) {
    constexpr int m = decltype(mc)::value;
    if constexpr (m == 0) wait_lgkm<4>();
    if constexpr (m == 4 || m == 29) wait_lgkm<0>();
    if constexpr (m == 25) wait_lgkm<8>();
    SBAR();
    mm(mc);
    hook(mc);
    SBAR();
    if constexpr (m >= 16 && m < 24) { vlo[m - 16] = tr_read((((m - 16) & 1) ? va1 : va0) + ((m - 16) >> 1) * 256);
                                       vhi[m - 16] = tr_read((((m - 16) & 1) ? va1 : va0) + ((m - 16) >> 1) * 256 + 4096); }
    if constexpr (m >= 43) A.kf[m - 43] = ld128(ka + ((m - 43) >> 2) * 4096 + ((m - 43) & 3) * 256);
    SBAR();
    if constexpr (m >= 6 && m < 14) E(IC(0), IC(m - 6));
    if constexpr (m == 14 || m == 15) E(IC(1), IC(m - 14));
    if constexpr (m == 20) E(IC(1), IC(2));
    if constexpr (m >= 24 && m < 29) E(IC(1), IC(m - 21));
    if constexpr (m >= 29 && m < 37) E(IC(2), IC(m - 29));
    if constexpr (m == 8 || m == 10 || m == 12 || m == 14) C(IC(0), IC((m - 8) / 2));
    if constexpr (m == 13) T1(IC(0));
    if constexpr (m == 15) T2(IC(0));
    if constexpr (m == 18) BR(IC(0));
    if constexpr (m == 17) C(IC(1), IC(0));
    if constexpr (m == 25 || m == 27 || m == 29) C(IC(1), IC((m - 23) / 2));
    if constexpr (m == 28) T1(IC(1));
    if constexpr (m == 30) T2(IC(1));
    if constexpr (m == 32) BR(IC(1));
    if constexpr (m == 31 || m == 33 || m == 35 || m == 37) C(IC(2), IC((m - 31) / 2));
    if constexpr (m == 37) T1(IC(2));
    if constexpr (m == 38) T2(IC(2));
    if constexpr (m == 41) BR(IC(2));
    SBAR();
  };
  for_seq(gap, std::make_integer_sequence<int, 51>{});
#undef IC
}
DI void att_init(Att& A, f32x4 (&acc)[8][3], bool odd) {
#pragma unroll
  for (int a = 0; a < 4; ++a)
#pragma unroll
    for (int q = 0; q < 3; ++q) {
      A.q[a][q] = __builtin_bit_cast(bf16x8, pack_pair(acc[2 * a][q] * QSCALE, acc[2 * a + 1][q] * QSCALE, odd));
      A.o[2 * a][q] = f32x4{0.f, 0.f, 0.f, 0.f}; A.o[2 * a + 1][q] = f32x4{0.f, 0.f, 0.f, 0.f};
    }
#pragma unroll
  for (int q = 0; q < 3; ++q) { A.l[q] = f32x4{0.f, 0.f, 0.f, 0.f}; A.nm[q] = f32x4{0.f, 0.f, 0.f, 0.f}; }
  A.ones = u32x4{0x3f803f80u, 0x3f803f80u, 0x3f803f80u, 0x3f803f80u}; asm volatile("" : "+v"(A.ones));
}
DI void att_read_k(Att& A, unsigned ka) {
#pragma unroll
  for (int i = 0; i < 8; ++i) A.kf[i] = ld128(ka + (i >> 2) * 4096 + (i & 3) * 256);
  SBAR(); wait_lgkm<0>(); SBAR();
}
DI void att_store_o(Att& A, unsigned obuf_lane) {
#pragma unroll
  for (int q = 0; q < 3; ++q) {
    const float inv = __builtin_amdgcn_rcpf(A.l[q][0]);
#pragma unroll
    for (int a = 0; a < 4; ++a) *LDS_PTR(u32x4, obuf_lane + q * 4096 + a * 1024) = pack_raw(A.o[2 * a][q] * inv, A.o[2 * a + 1][q] * inv);
  }
  wait_lgkm0();
}

DI void publish(u64* fl  , unsigned j, unsigned w, u64 tok, unsigned lane) {
  wait_vm<0>();
  if (lane < 2 && 2 * w + lane != j) __hip_atomic_store((gu64*)(fl + (2 * w + lane) * 16 + j), tok, __ATOMIC_RELAXED, __HIP_MEMORY_SCOPE_AGENT);
}
DI void reduce_out(const char* part  , u64* fl  , char* out  , unsigned j, unsigned w, u64 tok, unsigned lane) {
  const u32x4 srd = make_srd(part + (j >> 1) * 12288 + ((j & 1) * 2 + (w & 1)) * 3072, 7 * 49152 + 3072);
  gu64* f = (gu64*)(fl + j * 16 + ((lane & 7) ^ j));
  const unsigned vo = lane * 16, ol = ROWN + w * 3072 + vo;
  u32x4 v[8][3]; f32x2 ps[4][12];
#pragma unroll
  for (int i = 1; i < 8; ++i) asm volatile("" : "=v"(v[i][0]), "=v"(v[i][1]), "=v"(v[i][2]));
#pragma unroll
  for (int i = 0; i < 4; ++i)
#pragma unroll
    for (int e = 0; e < 12; ++e) asm volatile("" : "=v"(ps[i][e]));
  u64 x = __hip_atomic_load(f, __ATOMIC_RELAXED, __HIP_MEMORY_SCOPE_AGENT);
  lds_wait_ge(W_SENT, 4);
  unsigned asked = 1, summed = 0;
#pragma unroll 1
  for (unsigned spin = 0; spin < (1u << 17); ++spin) {
    asm volatile("s_waitcnt vmcnt(0)" : "+v"(x) :: "memory");
    const unsigned landed = asked, nw = (unsigned)__ballot(x == tok) & 255u & ~asked;
#pragma unroll
    for (int i = 1; i < 8; ++i)
      if (nw & (1u << i))
        asm volatile("buffer_load_dwordx4 %0, %3, %4, %5 offen sc1\n\tbuffer_load_dwordx4 %1, %3, %4, %5 offen offset:1024 sc1\n\tbuffer_load_dwordx4 %2, %3, %4, %5 offen offset:2048 sc1"
                     : "+v"(v[i][0]), "+v"(v[i][1]), "+v"(v[i][2]) : "v"(vo), "s"(srd), "s"((i ^ j) * 49152u) : "memory");
    asked |= nw;
    if (asked != 255u) x = __hip_atomic_load(f, __ATOMIC_RELAXED, __HIP_MEMORY_SCOPE_AGENT);
    const unsigned pairs = landed & (landed >> 1) & 0x55u & ~summed;
#pragma unroll
    for (int i = 3; i >= 0; --i)
      if (pairs & (1u << (2 * i))) {
        if (i == 0) { v[0][0] = *LDS_PTR(u32x4, ol); v[0][1] = *LDS_PTR(u32x4, ol + 1024); v[0][2] = *LDS_PTR(u32x4, ol + 2048); }
        asm volatile("" : "+v"(v[2 * i][0]), "+v"(v[2 * i][1]), "+v"(v[2 * i][2]), "+v"(v[2 * i + 1][0]), "+v"(v[2 * i + 1][1]), "+v"(v[2 * i + 1][2]));
#pragma unroll
        for (int e = 0; e < 12; ++e) { const unsigned a = v[2 * i][e >> 2][e & 3], c = v[2 * i + 1][e >> 2][e & 3];
          ps[i][e] = f32x2{__uint_as_float(a << 16), __uint_as_float(a & 0xffff0000u)} + f32x2{__uint_as_float(c << 16), __uint_as_float(c & 0xffff0000u)}; }
      }
    summed |= pairs;
    if (landed == 255u) break;
  }
  wait_vm<0>();
  const __amdgpu_buffer_rsrc_t ro = __builtin_amdgcn_make_buffer_rsrc(out + j * 128 + (w & 1) * 64, 0, 47 * 2048 + 64, 0x00020000);
#pragma unroll
  for (int q = 0; q < 3; ++q) {
    f32x2 s[4];
#pragma unroll
    for (int e = 0; e < 4; ++e) s[e] = (ps[0][4 * q + e] + ps[1][4 * q + e]) + (ps[2][4 * q + e] + ps[3][4 * q + e]);
    __builtin_amdgcn_raw_buffer_store_b128(pack_pair(f32x4{s[0][0], s[0][1], s[1][0], s[1][1]}, f32x4{s[2][0], s[2][1], s[3][0], s[3][1]}, (lane >> 4) & 1),
                                           ro, (q * 16 + (lane & 15)) * 2048 + (lane >> 4) * 16, 0, 16);
  }
  if (lds_add_rtn(W_RED, 1) == 3 && lane > 0 && lane < 8) __hip_atomic_store(f, ~tok, __ATOMIC_RELAXED, __HIP_MEMORY_SCOPE_AGENT);
}
DI u64 get_token() {
  lds_wait_ge(W_TOKV, 1);
  const unsigned lo = lds_peek(W_TOK), hi = lds_peek(W_TOK + 4);
  return ((u64)(unsigned)__builtin_amdgcn_readfirstlane(hi) << 32) | (unsigned)__builtin_amdgcn_readfirstlane(lo);
}
template <int N, int SH> DI void touch_rows(const char* p0, unsigned lane) {
  const u32x4 sr = make_srd(p0, ((N - 1) << SH) + 1024);
#pragma unroll
  for (int i = 0; i < N; ++i) dma<0, 0>(sr, lane * 16, (unsigned)i << SH);
}
template <int NT> DI void load_x(const char* x, unsigned w, unsigned r) {
  asm volatile("" : "+s"(w));
  const u32x4 sx = make_srd(x, 49152);
  const unsigned v = src_lane<10>(fresh_lane());
  const unsigned v0 = v ^ (((3 * w) & 2) << 4), v1 = (v ^ (((3 * w + 1) & 2) << 4)) + 3072, v2 = (v ^ (((3 * w + 2) & 2) << 4)) + 6144;
#pragma unroll
  for (int i = 0; i < 4; ++i) { const unsigned kq = i ^ r, so = kq * 256 + w * 12288;
    set_m0(RX + kq * 12288 + w * 3072); dma<0, NT>(sx, v0, so); dma<1024, NT>(sx, v1, so); dma<2048, NT>(sx, v2, so); }
}

__global__ __launch_bounds__(512, 2) void fused_k(Params p) {
  __shared__ __attribute__((aligned(1024))) char smem[LDS_BYTES];
  asm volatile("" :: "v"((LDS_AS char*)smem));
  const unsigned tid = threadIdx.x;
  unsigned lane;
#define NEWLANE() (lane = fresh_lane())
  const unsigned wid = __builtin_amdgcn_readfirstlane(tid >> 6), w = wid & 3;
  const unsigned j = blockIdx.x & 7, b = blockIdx.x >> 3, h = 4 * j + w, rot = b >> 1;
  const bool late = b & 1;
  const unsigned t0 = now_ticks();
  asm volatile("" :: "s"(p.x[0]), "s"(p.x[1]), "s"(p.k[0]), "s"(p.k[1]), "s"(p.v[0]), "s"(p.v[1]), "s"(p.wq[0]), "s"(p.wq[1]), "s"(p.wo[0]), "s"(p.wo[1]),
               "s"(p.part), "s"(p.flags), "s"(p.out), "s"(t0));
  if (tid < 64) *LDS_PTR(unsigned, RS + tid * 4) = 0;
  wait_lgkm0();
  __builtin_amdgcn_s_barrier();
  GemmCfg G;
  const unsigned ring = RA + w * 8192, bring = RB + w * 16384;
  if (!late) {
    if (wid < 4) { gemm_cfg<13>(G, p.wq[1] + h * 256, ring, 0, rot, 0, NEWLANE()); w_step<13>(ring, G.srd, G, 0, rot); }
    else load_x<0>(p.x[1] + (size_t)b * 49152, w, rot >> 2);
  }
  unsigned role = wid;
#define ROLE_B() ({ asm volatile("" : "+s"(role)); role < 4; })
  f32x4 acc[8][3];
  Att A;
  u64* const epoch = p.flags + 256 * 16 + b * 16;
  const unsigned turn = rot >= 8 ? TURNS2 : late ? TURNS1 : TURNS0;
  const unsigned cb = h, ca = b * 8 + j;
  const u32x4 sk = make_srd(p.k[1] + (size_t)(b * 32 + h) * 131072, 131072), sv = make_srd(p.v[1] + (size_t)(b * 32 + h) * 131072, 131072);
  auto kv0 = [&](int i, unsigned s0) INL {
    const unsigned so = (cb & 15) * 8192 + (i & 1) * 4096;
    if (i < 2) dma4(bring + i * 4096, sk, s0, s0, s0 ^ 32u, s0 ^ 32u, so); else dma4(bring + i * 4096, sv, s0, s0 ^ 16u, s0, s0 ^ 16u, so); };

  if (ROLE_B()) {
    if (late) {
      wait_until(t0, T_LATE);
      const unsigned s0 = src_lane<8>(NEWLANE());
      kv0(0, s0); kv0(1, s0); kv0(2, s0); kv0(3, s0);
      lds_wait_ge(W_GO, 4);
    }
  } else {
    NEWLANE();
    if (late) {
      wait_until(t0, T_LATE);
      __builtin_amdgcn_s_setprio(1);
      load_x<0>(p.x[1] + (size_t)b * 49152, w, 0);
      const unsigned r0 = (rot * 4 + w) * 8;
      set_m0(RDUMMY);
      touch_rows<3, 10>(p.x[0] + (size_t)(b - 1) * 49152 + (j * 12 + w * 3) * 1024, lane);
      touch_rows<8, 13>(p.wq[0] + (size_t)r0 * 8192 + j * 1024, lane);
      touch_rows<8, 10>(p.wo[0] + (size_t)(512 * j + r0) * 1024, lane);
      touch_rows<8, 10>(p.wo[1] + (size_t)(512 * j + r0) * 1024, lane);
      lds_add(W_GO, 1);
      wait_vm<27>(); lds_add(W_XB, 0x01010101u);
    } else {
      const unsigned r = rot >> 2;
      wait_vm<9>(); lds_add(W_XB, 1u << (8 * r));
      wait_vm<6>(); lds_add(W_XB, 1u << (8 * (r ^ 1)));
      wait_vm<3>(); lds_add(W_XB, 1u << (8 * (r ^ 2)));
      wait_vm<0>(); lds_add(W_XB, 1u << (8 * (r ^ 3)));
    }
    __builtin_amdgcn_s_setprio(1);
    lds_wait_ge(W_BQP, 4);
    load_x<1>(p.x[0] + (size_t)b * 49152, w, 0);
    if (w == 0) {
      const u64 tk = __hip_atomic_load((gu64*)epoch, __ATOMIC_RELAXED, __HIP_MEMORY_SCOPE_AGENT) + 0x9E3779B97F4A7C15ull;
      if (lane == 0) *LDS_PTR(u64, W_TOK) = tk;
      wait_lgkm0();
      lds_add(W_TOKV, 1);
    }
    wait_vm<0>();
    lds_add(W_ABAR, 1);
    wait_until(t0, T_QA + T_STEP * (late ? 8 + rot : rot < 8 ? rot : 16 + rot));
    lds_add(W_PAUSE, w == 0 ? 256u + (turn & 255) : 256u);
    lds_wait_ge(W_ABAR, 4);
  }

  { const bool rb = ROLE_B(), early = rb && !late;
    NEWLANE();
    gemm_cfg<13>(G, (rb ? p.wq[1] : p.wq[0]) + h * 256, ring, rd_rows(lane) + RX, rot, W_XB, lane);
    if (!early) w_step<13>(ring, G.srd, G, 0, rot);
    const unsigned s0 = src_lane<8>(lane);
    gemm<13, 2, 12288>(acc, G, [&](int i) INL {
      if (i == -1) { if (!rb) lds_add(W_PAUSE, 0u - 256u); }
      if (i == 0 || i == 3) { if (early) kv0(i / 3, s0); }
      if (i == 4) lds_add(rb ? W_BQP : W_AFREE, rb ? 1u : 1u << (8 * w));
    });
    NEWLANE();
    att_init(A, acc, (lane >> 4) & 1); }

  bool served = false;
  auto serve = [&](unsigned s0, unsigned s16, unsigned s32) INL {
    const u32x4 ak = make_srd(p.k[0] + (size_t)(b * 8 + j) * 131072, 131072), av = make_srd(p.v[0] + (size_t)(b * 8 + j) * 131072, 131072);
#pragma unroll 1
    for (unsigned c = 4 * w; c < 4 * w + 4; ++c) {
      lds_wait_all_ge(W_AFREE, c < NSA ? 1 : c - NSA + 2);
      const unsigned so = ((c + ca) & 15) * 8192, slot = RX + (c % NSA) * 16384;
      dma4(slot, ak, s0, s0, s32, s32, so); dma4(slot + 4096, ak, s0, s0, s32, s32, so + 4096);
      dma4(slot + 8192, av, s0, s16, s0, s16, so); dma4(slot + 12288, av, s0, s16, s0, s16, so + 4096);
      if ((c & 3) == 3) lds_add(W_PAUSE, (((turn >> (8 * w)) >> 8) & 255) - ((turn >> (8 * w)) & 255));
      if (c & 3) { wait_vm<16>(); if ((c & 3) == 1) lds_wait_ge(W_AFULL, c - 1); lds_add(W_AFULL, 1); }
    }
    wait_vm<0>(); lds_add(W_AFULL, 1);
    served = true;
  };
  float floorv = -__builtin_inff(); unsigned force = 0;
  if (ROLE_B()) {
    NEWLANE();
    const unsigned s0 = src_lane<8>(lane), s16 = s0 ^ 16u, s32 = s0 ^ 32u;
    const unsigned ka = rd_rows(lane) + bring, va0 = rd_cols(lane) + bring + 8192, va1 = va0 ^ 8u;
    if (!late) { kv0(2, s0); kv0(3, s0); }
    wait_vm<8>();
    att_read_k(A, ka);
    const unsigned pmask = 0xff00u | (17u << w);
    auto rest = [&]() INL {
      if (lds_wait_clear_or(W_PAUSE, pmask, 16u << w) & (16u << w)) { serve(s0, s16, s32); lds_wait_clear_or(W_PAUSE, pmask, 0); }
    };
    unsigned pz = 0;
#pragma unroll 1
    for (unsigned c = 0; c < 16; ++c) {
      const unsigned so = ((c + 1 + cb) & 15) * 8192, so2 = so + 4096; unsigned ps = 0;
      const bool more = c != 15;
      att_chunk(A, floorv, force, va0, va1, ka, [&](auto mc) {
        constexpr int m = decltype(mc)::value;
        if constexpr (m == 44 || m == 30) pz = lds_peek(W_PAUSE);
        if constexpr (m == 1 || m == 35) ps = to_sgpr(pz);
        if constexpr (m == 4 || m == 38) { if (more) {
          if (__builtin_expect(ps & pmask, 0)) rest();
          if (m == 4) dma4(bring, sk, s0, s0, s32, s32, so); else dma4(bring + 8192, sv, s0, s16, s0, s16, so); } }
        if constexpr (m == 5 || m == 40) { if (more) { if (m == 5) dma4(bring + 4096, sk, s0, s0, s32, s32, so2); else dma4(bring + 12288, sv, s0, s16, s0, s16, so2); } }
        if constexpr (m == 16) { wait_vm<8>(); if (!more) wait_vm<0>(); }
        if constexpr (m == 43) wait_vm<8>();
      });
      floorv = 0.f; force = 1;
    }
  } else {
    NEWLANE();
    const unsigned kl = rd_rows(lane) + RX, vl = rd_cols(lane) + RX + 8192;
    lds_wait_ge(W_AFULL, 1);
    att_read_k(A, kl);
#pragma unroll 1
    for (unsigned c = 0; c < 16; ++c) {
      const unsigned need = c < 15 ? c + 2 : 16;
      const unsigned va0 = vl + (c % NSA) * 16384, va1 = va0 ^ 8u, ka = kl + ((c + 1) % NSA) * 16384;
      unsigned bz = 0, bs = 0;
      att_chunk(A, floorv, force, va0, va1, ka, [&](auto mc) {
        constexpr int m = decltype(mc)::value;
        if constexpr (m == 36) bz = lds_peek(W_AFULL);
        if constexpr (m == 39) lds_add(W_AFREE, 1u << (8 * w));
        if constexpr (m == 40) bs = to_sgpr(bz);
        if constexpr (m == 42) { if (__builtin_expect(bs < need, 0)) lds_wait_ge(W_AFULL, need); }
      });
      floorv = 0.f; force = 1;
    }
    lds_add(W_PAUSE, 256u);
    lds_wait_all_ge(W_AFREE, 17);
  }
  { const bool rb = ROLE_B();
    wait_lgkm0();
    NEWLANE();
    att_store_o(A, (rb ? bring : RX + w * 12288) + lane * 16);
    lds_add(rb ? W_OB : W_ABAR, rb ? 4u << (8 * w) : 1u); }

  if (ROLE_B()) {
    NEWLANE();
    if (!served) { lds_wait_set(W_PAUSE, 16u << w); const unsigned s0 = src_lane<8>(lane); serve(s0, s0 ^ 16u, s0 ^ 32u); }
    const u64 tok = get_token();
    lds_wait_ge(W_WDONE, 4);
    reduce_out(p.part + (size_t)((w >> 1) * 256 + b * 8) * 49152, p.flags + b * 128, p.out + (size_t)b * 98304 + (w >> 1) * 1024, j, w, tok, lane);
    if (j == 0 && w == 0 && lane == 0) __hip_atomic_store((gu64*)epoch, tok, __ATOMIC_RELAXED, __HIP_MEMORY_SCOPE_AGENT);
  } else {
    NEWLANE();
    gemm_cfg<10>(G, p.wo[0] + (size_t)j * 524288 + w * 256, ring, RX + lane * 16, b & 15, W_XB, lane);
    w_step<10>(ring, G.srd, G, 0, b & 15);
    lds_wait_ge(W_ABAR, 8);
    const u32x4 swb = make_srd(p.wo[1] + (size_t)j * 524288 + w * 256, (511u << 10) + 256u);
    gemm<10, 2, 12288>(acc, G, [&](int i) INL {
      if (i >= 0 && i < 8) {
        const unsigned so = w_off<10>(0, i >> 2, b & 3), so1 = w_odd<10>(so);
        if ((i & 3) == 0) { set_m0(ring + (i >> 2) * 4096); w_piece<0>(swb, G, so, so1); } else if ((i & 3) == 1) w_piece<1>(swb, G, so, so1);
        else if ((i & 3) == 2) w_piece<2>(swb, G, so, so1); else w_piece<3>(swb, G, so, so1); }
      if (i == 4) lds_add(W_ABAR, 1);
      if (i == 11) { lds_wait_ge(W_ABAR, 12); w_step<10>(RX + w * 8192, swb, G, 1, b & 3); lds_add(W_PAUSE, 0u - 256u); }
    });
    NEWLANE();
    char* const part = p.part + (size_t)((b * 8 + j) * 4 + w) * 12288;
    const unsigned sz0 = j == 2 * w ? 0u : 12288u, sz1 = j == 2 * w + 1 ? 0u : 12288u, pl = lane * 16;
    const __amdgpu_buffer_rsrc_t ra0 = __builtin_amdgcn_make_buffer_rsrc(part, 0, sz0, 0x00020000), ra1 = __builtin_amdgcn_make_buffer_rsrc(part, 0, sz1, 0x00020000);
    const __amdgpu_buffer_rsrc_t rb0 = __builtin_amdgcn_make_buffer_rsrc(part + (size_t)256 * 49152, 0, sz0, 0x00020000);
    const __amdgpu_buffer_rsrc_t rb1 = __builtin_amdgcn_make_buffer_rsrc(part + (size_t)256 * 49152, 0, sz1, 0x00020000);
    u32x4 pa[12];
#pragma unroll
    for (int f = 0; f < 12; ++f) { pa[f] = pack_raw(acc[2 * (f / 3)][f % 3], acc[2 * (f / 3) + 1][f % 3]); asm volatile("" : "+v"(pa[f])); }
#pragma unroll
    for (int e = 0; e < 2; ++e) if (j == 2 * w + e) {
#pragma unroll
      for (int f = 0; f < 6; ++f) *LDS_PTR(u32x4, ROWN + f * 1024 + pl) = pa[6 * e + f]; }
    auto put_a = [&](int f) INL { __builtin_amdgcn_raw_buffer_store_b128(pa[f], f < 6 ? ra0 : ra1, pl + f * 1024, 0, 16); };
    auto put_b = [&](int a) INL {
#pragma unroll
      for (int q = 0; q < 3; ++q) __builtin_amdgcn_raw_buffer_store_b128(pack_raw(acc[2 * a][q], acc[2 * a + 1][q]), a < 2 ? rb0 : rb1, pl + (a * 3 + q) * 1024, 0, 16);
      if (j == 2 * w + (a >> 1)) {
#pragma unroll
        for (int q = 0; q < 3; ++q) *LDS_PTR(u32x4, ROWN + 6144 + ((a & 1) * 3 + q) * 1024 + pl) = pack_raw(acc[2 * a][q], acc[2 * a + 1][q]); } };
    gemm_cfg<10>(G, p.wo[1] + (size_t)j * 524288 + w * 256, RX + w * 8192, RB + pl, b & 3, W_OB, lane);
    gemm<10, 4, 16384>(acc, G, [&](int i) INL {
      if (i == -1) lds_add(W_WDONE, 1);
      if (i >= 0 && i < 8) put_a(i);
      if (i == 8 || i == 9) { put_a(2 * i - 8); put_a(2 * i - 7); }
      if (i == 11 || i == 13 || i == 15) put_b((i - 11) / 2);
    });
    put_b(3);
    wait_lgkm0(); lds_add(W_SENT, 1);
    publish(p.flags + b * 128, j, w, get_token(), lane);
  }
}

extern "C" void kernel_launch(void* const* d_in, const int* in_sizes, int n_in, void* d_out, int out_size, void* d_ws, size_t ws_size, hipStream_t stream) {
  Params p;
  for (int br = 0; br < 2; ++br) {
    p.x[br] = (const char*)d_in[br * 5 + 0]; p.k[br] = (const char*)d_in[br * 5 + 1]; p.v[br] = (const char*)d_in[br * 5 + 2];
    p.wq[br] = (const char*)d_in[br * 5 + 3]; p.wo[br] = (const char*)d_in[br * 5 + 4];
  }
  p.part = (char*)d_ws; p.flags = (u64*)((char*)d_ws + (32u << 20)); p.out = (char*)d_out;
  fused_k<<<256, 512, 0, stream>>>(p);
}
```

```cpp
#include <hip/hip_runtime.h>
#include <stdint.h>
#include <utility>

typedef __bf16 bf16x8 __attribute__((ext_vector_type(8)));
typedef __bf16 bf16x4 __attribute__((ext_vector_type(4)));
typedef __bf16 bf16x2 __attribute__((ext_vector_type(2)));
typedef float f32x4 __attribute__((ext_vector_type(4)));
typedef float f32x2 __attribute__((ext_vector_type(2)));
typedef unsigned u32x4 __attribute__((ext_vector_type(4)));
typedef unsigned long long u64;
typedef __attribute__((address_space(1))) u64 gu64;
#define LDS_AS __attribute__((address_space(3)))
#define LDS_PTR(T, a) ((LDS_AS T*)(uintptr_t)(a))
#define DI __device__ __forceinline__
#define INL __attribute__((always_inline))
#define SBAR() __builtin_amdgcn_sched_barrier(0)

constexpr unsigned T_LATE = 195;
constexpr unsigned TURNS0 = 0x8f4f2f1fu, TURNS1 = 0x8d4e271bu, TURNS2 = 0x894c2613u;

constexpr unsigned RB = 0;
constexpr unsigned RX = 65536;
constexpr unsigned RA = 114688;
constexpr unsigned ROWN = RX + 32768;
constexpr unsigned RK0 = 147456;
constexpr unsigned RDUMMY = 155648;
constexpr unsigned RS = 163584, LDS_BYTES = 163840;
constexpr unsigned W_ABAR = RS, W_GO = RS + 16, W_BQP = RS + 32, W_PAUSE = RS + 48, W_OB = RS + 64, W_XB = RS + 96, W_TOKV = RS + 112, W_TOK = RS + 128  ,
                   W_SENT = RS + 144, W_AFULL = RS + 160, W_WDONE = RS + 176, W_AFREE = RS + 192, W_RED = RS + 208;
constexpr unsigned NSA = 4;
constexpr float QSCALE = 0.08838834764831845f * 1.4426950408889634f;
constexpr float BIAS = 7.0f;

struct Params {
  const char* x[2]; const char* k[2]; const char* v[2]; const char* wq[2]; const char* wo[2];
  char* part; u64* flags; char* out;
};
struct Pace { unsigned t[32]; };

DI u32x4 make_srd(const char* p, unsigned bytes) { u32x4 s = {(unsigned)(uintptr_t)p, (unsigned)((uintptr_t)p >> 32), bytes, 0x00020000u}; return s; }
template <int N> DI void wait_vm() { asm volatile("s_waitcnt vmcnt(%0)" :: "i"(N) : "memory"); }
DI void wait_lgkm0() { asm volatile("s_waitcnt lgkmcnt(0)" ::: "memory"); }
template <int N> DI void wait_lgkm() { __builtin_amdgcn_s_waitcnt(0xC07F | (N << 8)); }
DI void set_m0(unsigned dst) { asm volatile("s_mov_b32 m0, %0\n\ts_nop 0" :: "s"(dst) : "memory"); }
template <int OFF, int NT> DI void dma(u32x4 srd, unsigned v, unsigned so) {
  if constexpr (NT) asm volatile("buffer_load_dwordx4 %0, %1, %3 offen offset:%2 nt lds" :: "v"(v), "s"(srd), "i"(OFF), "s"(so) : "memory");
  else asm volatile("buffer_load_dwordx4 %0, %1, %3 offen offset:%2 lds" :: "v"(v), "s"(srd), "i"(OFF), "s"(so) : "memory");
}
DI void dma4(unsigned dst, u32x4 srd, unsigned v0, unsigned v1, unsigned v2, unsigned v3, unsigned so) {
  set_m0(dst); dma<0, 1>(srd, v0, so); dma<1024, 1>(srd, v1, so); dma<2048, 1>(srd, v2, so); dma<3072, 1>(srd, v3, so);
}
DI bf16x4 tr_read(unsigned a) { return __builtin_amdgcn_ds_read_tr16_b64_v4bf16(LDS_PTR(bf16x4, a)); }
DI bf16x8 ld128(unsigned a) { return *LDS_PTR(bf16x8, a); }
DI bf16x8 cat(bf16x4 a, bf16x4 b) { return __builtin_shufflevector(a, b, 0, 1, 2, 3, 4, 5, 6, 7); }
DI f32x4 mfma(bf16x8 a, bf16x8 b, f32x4 c) { return __builtin_amdgcn_mfma_f32_16x16x32_bf16(a, b, c, 0, 0, 0); }
DI unsigned cvtpk(float lo, float hi) { f32x2 v = {lo, hi}; bf16x2 b = __builtin_convertvector(v, bf16x2); return __builtin_bit_cast(unsigned, b); }
DI u32x4 pack_raw(f32x4 t0, f32x4 t1) { return u32x4{cvtpk(t0[0], t0[1]), cvtpk(t0[2], t0[3]), cvtpk(t1[0], t1[1]), cvtpk(t1[2], t1[3])}; }
DI u32x4 pack_pair(f32x4 t0, f32x4 t1, bool odd) { const u32x4 r = pack_raw(t0, t1); return u32x4{odd ? r[2] : r[0], odd ? r[3] : r[1], odd ? r[0] : r[2], odd ? r[1] : r[3]}; }
DI unsigned fresh_lane() { unsigned l; asm volatile("v_mbcnt_lo_u32_b32 %0, -1, 0\n\tv_mbcnt_hi_u32_b32 %0, -1, %0" : "=v"(l)); return l; }
template <bool MAX> DI float across_groups(float d) {
  auto r = __builtin_amdgcn_permlane16_swap(__float_as_uint(d), __float_as_uint(d), false, false);
  d = MAX ? fmaxf(__uint_as_float(r[0]), __uint_as_float(r[1])) : __uint_as_float(r[0]) + __uint_as_float(r[1]);
  auto u = __builtin_amdgcn_permlane32_swap(__float_as_uint(d), __float_as_uint(d), false, false);
  return MAX ? fmaxf(__uint_as_float(u[0]), __uint_as_float(u[1])) : __uint_as_float(u[0]) + __uint_as_float(u[1]);
}

DI void lds_add(unsigned addr, unsigned val) {
  asm volatile("s_mov_b64 exec, 1\n\tds_add_u32 %0, %1\n\ts_mov_b64 exec, -1" :: "v"(addr), "v"(val) : "memory");
}
DI unsigned lds_add_rtn(unsigned addr, unsigned val) {
  unsigned v, r;
  asm volatile("s_mov_b64 exec, 1\n\tds_add_rtn_u32 %0, %2, %3\n\ts_waitcnt lgkmcnt(0)\n\tv_readfirstlane_b32 %1, %0\n\ts_mov_b64 exec, -1\n\ts_nop 0\n\tv_mov_b32 %0, %1\n\ts_nop 0"
               : "=&v"(v), "=&s"(r) : "v"(addr), "v"(val) : "memory");
  return v;
}
#define LDS_SPIN(TEST, SLEEP) "1:\n\tds_read_b32 %0, %3\n\ts_waitcnt lgkmcnt(0)\n\tv_readfirstlane_b32 %1, %0\n\ts_nop 0\n\t" TEST "s_sleep " SLEEP "\n\ts_sub_u32 %2, %2, 1\n\ts_cbranch_scc0 1b\n2:"
DI void lds_wait_ge(unsigned addr, unsigned target) {
  unsigned v, sv, cnt = 1u << 18;
  asm volatile(LDS_SPIN("s_cmp_ge_u32 %1, %4\n\ts_cbranch_scc1 2f\n\t", "1") : "=&v"(v), "=&s"(sv), "+&s"(cnt) : "v"(addr), "s"(target) : "memory", "scc");
}
DI void lds_wait_all_ge(unsigned addr, unsigned n) {
  unsigned v, sv, cnt = 1u << 18;
  asm volatile(LDS_SPIN("s_add_u32 %1, %1, %4\n\ts_andn2_b32 %1, 0x80808080, %1\n\ts_cbranch_scc0 2f\n\t", "1")
               : "=&v"(v), "=&s"(sv), "+&s"(cnt) : "v"(addr), "s"((128u - n) * 0x01010101u) : "memory", "scc");
}
DI void lds_wait_set(unsigned addr, unsigned set) {
  unsigned v, sv, cnt = 1u << 18;
  asm volatile(LDS_SPIN("s_and_b32 %1, %1, %4\n\ts_cbranch_scc1 2f\n\t", "2") : "=&v"(v), "=&s"(sv), "+&s"(cnt) : "v"(addr), "s"(set) : "memory", "scc");
}
DI unsigned lds_wait_clear_or(unsigned addr, unsigned mask, unsigned set) {
  unsigned v, sv, cnt = 1u << 18;
  asm volatile(LDS_SPIN("s_and_b32 %1, %1, %4\n\ts_cbranch_scc0 2f\n\ts_and_b32 %1, %1, %5\n\ts_cbranch_scc1 2f\n\t", "2")
               : "=&v"(v), "=&s"(sv), "+&s"(cnt) : "v"(addr), "s"(mask), "s"(set) : "memory", "scc");
  return __builtin_amdgcn_readfirstlane(v);
}
DI unsigned lds_peek(unsigned addr) { return *(volatile LDS_AS unsigned*)(uintptr_t)addr; }
DI unsigned to_sgpr(unsigned v) { unsigned r = __builtin_amdgcn_readfirstlane(v); asm volatile("" : "+s"(r)); return r; }
DI unsigned now_ticks() { return (unsigned)__builtin_amdgcn_s_memrealtime(); }
DI void wait_until(unsigned t0, unsigned ticks) {
#pragma unroll 1
  while (now_ticks() - t0 < ticks) __builtin_amdgcn_s_sleep(4);
}

template <int SH> DI unsigned src_lane(unsigned lane) { return (((lane >> 2) & 3) << SH) + (lane >> 4) * 64 + (lane & 3) * 16; }
DI unsigned rd_rows(unsigned lane) { const unsigned g = lane >> 4, m = lane & 15; return (m >> 2) * 1024 + (m & 3) * 64 + ((g ^ ((m >> 2) & 2)) << 4); }
DI unsigned rd_cols(unsigned lane) { const unsigned g = lane >> 4, q = (lane >> 2) & 3, pp = lane & 3; return g * 1024 + q * 64 + ((pp ^ (g & 1)) << 4) + 8 * (pp & 1); }

struct Frag { bf16x4 lo[8], hi[8]; bf16x8 b[3]; };
struct GemmCfg { u32x4 srd; unsigned wv[4]; unsigned ring, ga0, ga1, bl, rot, gv; };
template <int SH> DI void gemm_cfg(GemmCfg& G, const char* w, unsigned ring, unsigned bl, unsigned rot, unsigned gate, unsigned lane) {
  constexpr unsigned U = (8u << SH) - 1024u;
  const unsigned w0 = src_lane<SH>(lane);
  G.srd = make_srd(w, (511u << SH) + 256u);
  G.wv[0] = w0; G.wv[1] = (w0 ^ 16u) + U; G.wv[2] = w0 + 2 * U; G.wv[3] = (w0 ^ 16u) + 3 * U;
  G.ring = ring; G.ga0 = rd_cols(lane) + ring; G.ga1 = G.ga0 ^ 8u; G.bl = bl; G.rot = rot; G.gv = gate; asm volatile("" : "+v"(G.gv));
}
template <int SH> DI unsigned w_off(unsigned s, unsigned t, unsigned rot) { return (((s ^ rot) << 5) + 4u * t) << SH; }
template <int SH> DI unsigned w_odd(unsigned so) { return SH == 10 ? so ^ (4u << SH) : so; }
template <int U> DI void w_piece(u32x4 srd, const GemmCfg& G, unsigned so, unsigned so1) { dma<1024 * U, 0>(srd, G.wv[U], (U & 1) ? so1 : so); }
template <int SH> DI void w_step(unsigned dst, u32x4 srd, const GemmCfg& G, unsigned s, unsigned rot) {
#pragma unroll
  for (int t = 0; t < 2; ++t) { const unsigned so = w_off<SH>(s, t, rot), so1 = w_odd<SH>(so);
    set_m0(dst + t * 4096); w_piece<0>(srd, G, so, so1); w_piece<1>(srd, G, so, so1); w_piece<2>(srd, G, so, so1); w_piece<3>(srd, G, so, so1); }
}
template <int SH, int NS, int BK, int MODE, int PAR, class Tail>
DI void gstep(f32x4 (&acc)[8][3], const Frag& c, Frag& n, const GemmCfg& G, unsigned s, Tail tail) {
  constexpr unsigned BS = SH == 13 ? 256 : 1024, SL = NS == 4 && PAR == 0 ? RA - RX : 0;
  u32x4 srd = G.srd;
  if (NS == 4 && MODE == 1 && PAR == 0) srd[2] = s < 14 ? srd[2] : 0u;
#define M(k) do { if (MODE != 0) acc[4 * t + (k) / 3][(k) % 3] = mfma(cat(c.lo[4 * t + (k) / 3], c.hi[4 * t + (k) / 3]), c.b[(k) % 3], acc[4 * t + (k) / 3][(k) % 3]); SBAR(); } while (0)
#define RD(i) d[i] = tr_read((((i) & 1) ? G.ga1 : G.ga0) + SL + t * 4096 + ((i) >> 1) * 256)
#define PC(u) do { if (MODE != 2) w_piece<u>(srd, G, so, so1); else { if (u == 0 && t == 0) wait_vm<4 * NS - 8>(); tail(4 * t + u); } } while (0)
  const unsigned se = s ^ G.rot, gbit = 4u << (8 * (se >> 2));
  unsigned xb = 0, gz = 0, gs = 0;
#pragma unroll
  for (int t = 0; t < 2; ++t) {
    bf16x4* d = t ? n.hi : n.lo;
    if (MODE == 2 && t == 0) tail(-1);
    if (MODE != 2 || t == 0) wait_vm<4 * NS - 4>();
    SBAR();
    M(0); RD(0); RD(1); RD(2); SBAR();
    M(1); RD(3); RD(4); RD(5); SBAR();
    M(2); RD(6); RD(7); SBAR();
    M(3);
    if (t == 0 && PAR == 0) gz = lds_peek(G.gv);
    if (t == 1) { if (PAR == 0) { if (__builtin_expect(!(gs & gbit), 0)) lds_wait_set(G.gv, gbit); } n.b[0] = ld128(xb); n.b[1] = ld128(xb + 4096); }
    SBAR();
    const unsigned so = w_off<SH>((s + NS / 2) & 15u, t, G.rot), so1 = w_odd<SH>(so);
    M(4); if (t == 1) n.b[2] = ld128(xb + 8192); SBAR();
    if (MODE != 2) asm volatile("" :: "s"(so), "s"(so1));
    M(5); SBAR();
    M(6); if (MODE == 0) set_m0(G.ring + SL + t * 4096); if (MODE == 1) asm volatile("s_mov_b32 m0, %0" :: "s"(G.ring + SL + t * 4096) : "memory"); SBAR();
    M(7); asm volatile("" ::: "memory"); wait_lgkm<0>(); SBAR(); PC(0); SBAR();
    M(8); PC(1); if (t == 0 && PAR == 0) gs = to_sgpr(gz); SBAR();
    M(9); PC(2); if (t == 0) { xb = G.bl + ((se & 3) * BS + (se >> 2) * BK); asm volatile("" :: "v"(xb)); } SBAR();
    M(10); PC(3); SBAR();
    M(11);
  }
#undef PC
#undef M
#undef RD
}
template <int SH, int NS, int BK, class Tail>
DI void gemm(f32x4 (&acc)[8][3], const GemmCfg& G, Tail tail) {
#pragma unroll
  for (int a = 0; a < 8; ++a)
#pragma unroll
    for (int q = 0; q < 3; ++q) acc[a][q] = f32x4{0.f, 0.f, 0.f, 0.f};
  Frag X, Y;
  gstep<SH, NS, BK, 0, 0>(acc, Y, X, G, 0, tail);
#pragma unroll 1
  for (unsigned s = 1; s < 15; s += 2) {
    gstep<SH, NS, BK, 1, 1>(acc, X, Y, G, s, tail);
    gstep<SH, NS, BK, 1, 0>(acc, Y, X, G, s + 1, tail);
  }
  gstep<SH, NS, BK, 2, 1>(acc, X, Y, G, 15, tail);
#pragma unroll
  for (int i = 0; i < 8; ++i) {
#pragma unroll
    for (int q = 0; q < 3; ++q) acc[i][q] = mfma(cat(Y.lo[i], Y.hi[i]), Y.b[q], acc[i][q]);
    SBAR(); tail(8 + i); SBAR();
  }
}

template <class F, int... Ms> DI void for_seq(F& f, std::integer_sequence<int, Ms...>) { (f(std::integral_constant<int, Ms>{}), ...); }
struct Att { bf16x8 q[4][3]; f32x4 o[8][3]; f32x4 l[3]; f32x4 nm[3]; bf16x8 kf[8]; u32x4 ones; };

template <class Hook>
DI void att_chunk(Att& A, float floorv, unsigned force, unsigned va0, unsigned va1, unsigned ka, Hook hook) {
  f32x4 s[3][2]; u32x4 pw[3]; bf16x4 vlo[8], vhi[8]; float ex[3][8]; unsigned tt[3]; u64 big[3];
#define IC(n) std::integral_constant<int, (n)>{}
  auto mm = [&](auto mc) {
    constexpr int m = decltype(mc)::value;
    if constexpr (m < 24) { constexpr int q = m >> 3, kt = (m >> 2) & 1, i = m & 3; s[q][kt] = mfma(A.kf[4 * kt + i], A.q[i][q], i == 0 ? A.nm[q] : s[q][kt]); }
    else { constexpr int q = (m - 24) / 9, i = (m - 24) % 9;
      if constexpr (i == 0) A.l[q] = mfma(__builtin_bit_cast(bf16x8, A.ones), __builtin_bit_cast(bf16x8, pw[q]), A.l[q]);
      else A.o[i - 1][q] = mfma(cat(vlo[i - 1], vhi[i - 1]), __builtin_bit_cast(bf16x8, pw[q]), A.o[i - 1][q]); }
    SBAR();
  };
  auto E = [&](auto qc, auto ic) { constexpr int q = decltype(qc)::value, i = decltype(ic)::value;
    ex[q][i] = __builtin_amdgcn_exp2f(s[q][i >> 2][i & 3]); asm volatile("" :: "v"(ex[q][i])); SBAR(); };
  auto C = [&](auto qc, auto ic) { constexpr int q = decltype(qc)::value, i = decltype(ic)::value;
    const unsigned wd = cvtpk(ex[q][2 * i], ex[q][2 * i + 1]); asm volatile("" :: "v"(wd)); pw[q][i] = wd; SBAR(); };
  auto T1 = [&](auto qc) { constexpr int q = decltype(qc)::value; tt[q] = pw[q][0] | pw[q][1] | pw[q][2]; asm volatile("" : "+v"(tt[q])); SBAR(); };
  auto T2 = [&](auto qc) { constexpr int q = decltype(qc)::value;
    big[q] = __ballot(((tt[q] | pw[q][3]) & 0x40004000u) >= force); asm volatile("" : "+s"(big[q])); SBAR(); };
  auto BR = [&](auto qc) { constexpr int q = decltype(qc)::value;
    if (__builtin_expect(big[q] != 0, 0)) {
      float d = fmaxf(fmaxf(fmaxf(s[q][0][0], s[q][0][1]), fmaxf(s[q][0][2], s[q][0][3])), fmaxf(fmaxf(s[q][1][0], s[q][1][1]), fmaxf(s[q][1][2], s[q][1][3])));
      d = fmaxf(across_groups<true>(d) + BIAS, floorv);
      const float f = __builtin_amdgcn_exp2f(fminf(-d, 0.f));
#pragma unroll
      for (int r = 0; r < 4; ++r) { A.nm[q][r] -= d; A.l[q][r] *= f;
        pw[q][r] = cvtpk(__builtin_amdgcn_exp2f(s[q][r >> 1][2 * (r & 1)] - d), __builtin_amdgcn_exp2f(s[q][r >> 1][2 * (r & 1) + 1] - d)); }
      if (force) {
#pragma unroll
        for (int i = 0; i < 8; ++i) A.o[i][q] *= f; }
    }
    SBAR(); };
  auto gap = [&](auto mc) {
    constexpr int m = decltype(mc)::value;
    if constexpr (m == 0) wait_lgkm<4>();
    if constexpr (m == 4 || m == 29) wait_lgkm<0>();
    if constexpr (m == 25) wait_lgkm<8>();
    SBAR();
    mm(mc);
    hook(mc);
    SBAR();
    if constexpr (m >= 16 && m < 24) { vlo[m - 16] = tr_read((((m - 16) & 1) ? va1 : va0) + ((m - 16) >> 1) * 256);
                                       vhi[m - 16] = tr_read((((m - 16) & 1) ? va1 : va0) + ((m - 16) >> 1) * 256 + 4096); }
    if constexpr (m >= 43) A.kf[m - 43] = ld128(ka + ((m - 43) >> 2) * 4096 + ((m - 43) & 3) * 256);
    SBAR();
    if constexpr (m >= 6 && m < 14) E(IC(0), IC(m - 6));
    if constexpr (m == 14 || m == 15) E(IC(1), IC(m - 14));
    if constexpr (m == 20) E(IC(1), IC(2));
    if constexpr (m >= 24 && m < 29) E(IC(1), IC(m - 21));
    if constexpr (m >= 29 && m < 37) E(IC(2), IC(m - 29));
    if constexpr (m == 8 || m == 10 || m == 12 || m == 14) C(IC(0), IC((m - 8) / 2));
    if constexpr (m == 13) T1(IC(0));
    if constexpr (m == 15) T2(IC(0));
    if constexpr (m == 18) BR(IC(0));
    if constexpr (m == 17) C(IC(1), IC(0));
    if constexpr (m == 25 || m == 27 || m == 29) C(IC(1), IC((m - 23) / 2));
    if constexpr (m == 28) T1(IC(1));
    if constexpr (m == 30) T2(IC(1));
    if constexpr (m == 32) BR(IC(1));
    if constexpr (m == 31 || m == 33 || m == 35 || m == 37) C(IC(2), IC((m - 31) / 2));
    if constexpr (m == 37) T1(IC(2));
    if constexpr (m == 38) T2(IC(2));
    if constexpr (m == 41) BR(IC(2));
    SBAR();
  };
  for_seq(gap, std::make_integer_sequence<int, 51>{});
#undef IC
}
DI void att_init(Att& A, f32x4 (&acc)[8][3], unsigned lane) {
  const bool odd = (lane >> 4) & 1;
#pragma unroll
  for (int a = 0; a < 4; ++a)
#pragma unroll
    for (int q = 0; q < 3; ++q) {
      A.q[a][q] = __builtin_bit_cast(bf16x8, pack_pair(acc[2 * a][q] * QSCALE, acc[2 * a + 1][q] * QSCALE, odd));
      A.o[2 * a][q] = f32x4{0.f, 0.f, 0.f, 0.f}; A.o[2 * a + 1][q] = f32x4{0.f, 0.f, 0.f, 0.f};
    }
#pragma unroll
  for (int q = 0; q < 3; ++q) { A.l[q] = f32x4{0.f, 0.f, 0.f, 0.f}; A.nm[q] = f32x4{0.f, 0.f, 0.f, 0.f}; }
  const unsigned o1 = (lane & 15) ? 0u : 0x3f803f80u;
  A.ones = u32x4{o1, o1, o1, o1}; asm volatile("" : "+v"(A.ones));
}
DI void att_read_k(Att& A, unsigned ka) {
#pragma unroll
  for (int i = 0; i < 8; ++i) A.kf[i] = ld128(ka + (i >> 2) * 4096 + (i & 3) * 256);
  SBAR(); wait_lgkm<0>(); SBAR();
}
DI void att_store_o(Att& A, unsigned obuf_lane) {
#pragma unroll
  for (int q = 0; q < 3; ++q) {
    const float inv = __builtin_amdgcn_rcpf(across_groups<false>(A.l[q][0]));
#pragma unroll
    for (int a = 0; a < 4; ++a) *LDS_PTR(u32x4, obuf_lane + q * 4096 + a * 1024) = pack_raw(A.o[2 * a][q] * inv, A.o[2 * a + 1][q] * inv);
  }
  wait_lgkm0();
}

DI void publish(u64* fl  , unsigned j, unsigned w, u64 tok, unsigned lane) {
  wait_vm<0>();
  if (lane < 2 && 2 * w + lane != j) __hip_atomic_store((gu64*)(fl + (2 * w + lane) * 16 + j), tok, __ATOMIC_RELAXED, __HIP_MEMORY_SCOPE_AGENT);
}
DI void reduce_out(const char* part  , u64* fl  , char* out  , unsigned j, unsigned w, u64 tok, unsigned lane) {
  const u32x4 srd = make_srd(part + (j >> 1) * 12288 + ((j & 1) * 2 + (w & 1)) * 3072, 7 * 49152 + 3072);
  gu64* f = (gu64*)(fl + j * 16 + ((lane & 7) ^ j));
  const unsigned vo = lane * 16, ol = ROWN + w * 3072 + vo;
  u32x4 v[8][3]; f32x2 ps[4][12];
#pragma unroll
  for (int i = 1; i < 8; ++i) asm volatile("" : "=v"(v[i][0]), "=v"(v[i][1]), "=v"(v[i][2]));
#pragma unroll
  for (int i = 0; i < 4; ++i)
#pragma unroll
    for (int e = 0; e < 12; ++e) asm volatile("" : "=v"(ps[i][e]));
  u64 x = __hip_atomic_load(f, __ATOMIC_RELAXED, __HIP_MEMORY_SCOPE_AGENT);
  lds_wait_ge(W_SENT, 4);
  unsigned asked = 1, summed = 0;
#pragma unroll 1
  for (unsigned spin = 0; spin < (1u << 17); ++spin) {
    asm volatile("s_waitcnt vmcnt(0)" : "+v"(x) :: "memory");
    const unsigned landed = asked, nw = (unsigned)__ballot(x == tok) & 255u & ~asked;
#pragma unroll
    for (int i = 1; i < 8; ++i)
      if (nw & (1u << i))
        asm volatile("buffer_load_dwordx4 %0, %3, %4, %5 offen sc1\n\tbuffer_load_dwordx4 %1, %3, %4, %5 offen offset:1024 sc1\n\tbuffer_load_dwordx4 %2, %3, %4, %5 offen offset:2048 sc1"
                     : "+v"(v[i][0]), "+v"(v[i][1]), "+v"(v[i][2]) : "v"(vo), "s"(srd), "s"((i ^ j) * 49152u) : "memory");
    asked |= nw;
    if (asked != 255u) x = __hip_atomic_load(f, __ATOMIC_RELAXED, __HIP_MEMORY_SCOPE_AGENT);
    const unsigned pairs = landed & (landed >> 1) & 0x55u & ~summed;
#pragma unroll
    for (int i = 3; i >= 0; --i)
      if (pairs & (1u << (2 * i))) {
        if (i == 0) { v[0][0] = *LDS_PTR(u32x4, ol); v[0][1] = *LDS_PTR(u32x4, ol + 1024); v[0][2] = *LDS_PTR(u32x4, ol + 2048); }
        asm volatile("" : "+v"(v[2 * i][0]), "+v"(v[2 * i][1]), "+v"(v[2 * i][2]), "+v"(v[2 * i + 1][0]), "+v"(v[2 * i + 1][1]), "+v"(v[2 * i + 1][2]));
#pragma unroll
        for (int e = 0; e < 12; ++e) { const unsigned a = v[2 * i][e >> 2][e & 3], c = v[2 * i + 1][e >> 2][e & 3];
          ps[i][e] = f32x2{__uint_as_float(a << 16), __uint_as_float(a & 0xffff0000u)} + f32x2{__uint_as_float(c << 16), __uint_as_float(c & 0xffff0000u)}; }
      }
    summed |= pairs;
    if (landed == 255u) break;
  }
  wait_vm<0>();
  const __amdgpu_buffer_rsrc_t ro = __builtin_amdgcn_make_buffer_rsrc(out + j * 128 + (w & 1) * 64, 0, 47 * 2048 + 64, 0x00020000);
#pragma unroll
  for (int q = 0; q < 3; ++q) {
    f32x2 s[4];
#pragma unroll
    for (int e = 0; e < 4; ++e) s[e] = (ps[0][4 * q + e] + ps[1][4 * q + e]) + (ps[2][4 * q + e] + ps[3][4 * q + e]);
    __builtin_amdgcn_raw_buffer_store_b128(pack_pair(f32x4{s[0][0], s[0][1], s[1][0], s[1][1]}, f32x4{s[2][0], s[2][1], s[3][0], s[3][1]}, (lane >> 4) & 1),
                                           ro, (q * 16 + (lane & 15)) * 2048 + (lane >> 4) * 16, 0, 16);
  }
  if (lds_add_rtn(W_RED, 1) == 3 && lane > 0 && lane < 8) __hip_atomic_store(f, ~tok, __ATOMIC_RELAXED, __HIP_MEMORY_SCOPE_AGENT);
}
DI u64 get_token() {
  lds_wait_ge(W_TOKV, 1);
  const unsigned lo = lds_peek(W_TOK), hi = lds_peek(W_TOK + 4);
  return ((u64)(unsigned)__builtin_amdgcn_readfirstlane(hi) << 32) | (unsigned)__builtin_amdgcn_readfirstlane(lo);
}
template <int N, int SH> DI void touch_rows(const char* p0, unsigned lane) {
  const u32x4 sr = make_srd(p0, ((N - 1) << SH) + 1024);
#pragma unroll
  for (int i = 0; i < N; ++i) dma<0, 0>(sr, lane * 16, (unsigned)i << SH);
}
template <int NT> DI void load_x(const char* x, unsigned w, unsigned r) {
  asm volatile("" : "+s"(w));
  const u32x4 sx = make_srd(x, 49152);
  const unsigned v = src_lane<10>(fresh_lane());
  const unsigned v0 = v ^ (((3 * w) & 2) << 4), v1 = (v ^ (((3 * w + 1) & 2) << 4)) + 3072, v2 = (v ^ (((3 * w + 2) & 2) << 4)) + 6144;
#pragma unroll
  for (int i = 0; i < 4; ++i) { const unsigned kq = i ^ r, so = kq * 256 + w * 12288;
    set_m0(RX + kq * 12288 + w * 3072); dma<0, NT>(sx, v0, so); dma<1024, NT>(sx, v1, so); dma<2048, NT>(sx, v2, so); }
}

__global__ __launch_bounds__(512, 2) void fused_k(Params p, Pace pc) {
  __shared__ __attribute__((aligned(1024))) char smem[LDS_BYTES];
  asm volatile("" :: "v"((LDS_AS char*)smem));
  const unsigned tid = threadIdx.x;
  unsigned lane;
#define NEWLANE() (lane = fresh_lane())
  const unsigned wid = __builtin_amdgcn_readfirstlane(tid >> 6), w = wid & 3;
  const unsigned j = blockIdx.x & 7, b = blockIdx.x >> 3, h = 4 * j + w, rot = b >> 1;
  const bool late = b & 1;
  const unsigned t0 = now_ticks(), pace = pc.t[b];
  asm volatile("" :: "s"(p.x[0]), "s"(p.x[1]), "s"(p.k[0]), "s"(p.k[1]), "s"(p.v[0]), "s"(p.v[1]), "s"(p.wq[0]), "s"(p.wq[1]), "s"(p.wo[0]), "s"(p.wo[1]),
               "s"(p.part), "s"(p.flags), "s"(p.out), "s"(t0), "s"(pace));
  if (tid < 64) *LDS_PTR(unsigned, RS + tid * 4) = 0;
  wait_lgkm0();
  __builtin_amdgcn_s_barrier();
  GemmCfg G;
  const unsigned ring = RA + w * 8192, bring = RB + w * 16384;
  if (!late) {
    if (wid < 4) { gemm_cfg<13>(G, p.wq[1] + h * 256, ring, 0, rot, 0, NEWLANE()); w_step<13>(ring, G.srd, G, 0, rot); }
    else load_x<0>(p.x[1] + (size_t)b * 49152, w, rot >> 2);
  }
  unsigned role = wid;
#define ROLE_B() ({ asm volatile("" : "+s"(role)); role < 4; })
  f32x4 acc[8][3];
  Att A;
  u64* const epoch = p.flags + 256 * 16 + b * 16;
  const unsigned turn = (pace >> 26) == 0 ? TURNS0 : (pace >> 26) == 1 ? TURNS1 : TURNS2;
  const unsigned cb = h, ca = b * 8 + j;
  const u32x4 sk = make_srd(p.k[1] + (size_t)(b * 32 + h) * 131072, 131072), sv = make_srd(p.v[1] + (size_t)(b * 32 + h) * 131072, 131072);
  auto kv0 = [&](int i, unsigned s0) INL {
    const unsigned so = (cb & 15) * 8192 + (i & 1) * 4096;
    if (i < 2) dma4(bring + i * 4096, sk, s0, s0, s0 ^ 32u, s0 ^ 32u, so); else dma4(bring + i * 4096, sv, s0, s0 ^ 16u, s0, s0 ^ 16u, so); };

  if (ROLE_B()) {
    if (late) {
      wait_until(t0, T_LATE);
      const unsigned s0 = src_lane<8>(NEWLANE());
      kv0(0, s0); kv0(1, s0); kv0(2, s0); kv0(3, s0);
      lds_wait_ge(W_GO, 4);
    }
  } else {
    NEWLANE();
    if (late) {
      wait_until(t0, T_LATE);
      __builtin_amdgcn_s_setprio(1);
      load_x<0>(p.x[1] + (size_t)b * 49152, w, 0);
      const unsigned r0 = (rot * 4 + w) * 8;
      set_m0(RDUMMY);
      touch_rows<3, 10>(p.x[0] + (size_t)(b - 1) * 49152 + (j * 12 + w * 3) * 1024, lane);
      touch_rows<8, 13>(p.wq[0] + (size_t)r0 * 8192 + j * 1024, lane);
      touch_rows<8, 10>(p.wo[0] + (size_t)(512 * j + r0) * 1024, lane);
      touch_rows<8, 10>(p.wo[1] + (size_t)(512 * j + r0) * 1024, lane);
      lds_add(W_GO, 1);
      wait_vm<27>(); lds_add(W_XB, 0x01010101u);
    } else {
      const unsigned r = rot >> 2;
      wait_vm<9>(); lds_add(W_XB, 1u << (8 * r));
      wait_vm<6>(); lds_add(W_XB, 1u << (8 * (r ^ 1)));
      wait_vm<3>(); lds_add(W_XB, 1u << (8 * (r ^ 2)));
      wait_vm<0>(); lds_add(W_XB, 1u << (8 * (r ^ 3)));
    }
    __builtin_amdgcn_s_setprio(1);
    lds_wait_ge(W_BQP, 4);
    load_x<1>(p.x[0] + (size_t)b * 49152, w, 0);
    { const unsigned s0 = src_lane<8>(lane) ^ ((w & 1) << 5), so = (ca & 15) * 8192 + w * 2048;
      const u32x4 ak = make_srd(p.k[0] + (size_t)(b * 8 + j) * 131072, 131072);
      set_m0(RK0 + w * 2048); dma<0, 1>(ak, s0, so); dma<1024, 1>(ak, s0, so); }
    if (w == 0) {
      const u64 tk = __hip_atomic_load((gu64*)epoch, __ATOMIC_RELAXED, __HIP_MEMORY_SCOPE_AGENT) + 0x9E3779B97F4A7C15ull;
      if (lane == 0) *LDS_PTR(u64, W_TOK) = tk;
      wait_lgkm0();
      lds_add(W_TOKV, 1);
    }
    wait_vm<0>();
    lds_add(W_ABAR, 1);
    wait_until(t0, pace & 8191);
    lds_add(W_PAUSE, w == 0 ? 256u + (turn & 255) : 256u);
    lds_wait_ge(W_ABAR, 4);
  }

  { const bool rb = ROLE_B(), early = rb && !late;
    NEWLANE();
    gemm_cfg<13>(G, (rb ? p.wq[1] : p.wq[0]) + h * 256, ring, rd_rows(lane) + RX, rot, W_XB, lane);
    if (!early) w_step<13>(ring, G.srd, G, 0, rot);
    const unsigned s0 = src_lane<8>(lane);
    gemm<13, 2, 12288>(acc, G, [&](int i) INL {
      if (i == -1) { if (!rb) lds_add(W_PAUSE, 0u - 256u); }
      if (i == 0 || i == 3) { if (early) kv0(i / 3, s0); }
      if (i == 4) lds_add(rb ? W_BQP : W_AFREE, rb ? 1u : 1u << (8 * w));
    });
    att_init(A, acc, NEWLANE()); }

  bool served = false;
  auto serve = [&](unsigned s0, unsigned s16, unsigned s32) INL {
    const u32x4 ak = make_srd(p.k[0] + (size_t)(b * 8 + j) * 131072, 131072), av = make_srd(p.v[0] + (size_t)(b * 8 + j) * 131072, 131072);
#pragma unroll 1
    for (unsigned c = 4 * w; c < 4 * w + 4; ++c) {
      lds_wait_all_ge(W_AFREE, c < NSA ? 1 : c - NSA + 2);
      const unsigned so = ((c + ca) & 15) * 8192, slot = RX + (c % NSA) * 16384;
      if (c) { dma4(slot, ak, s0, s0, s32, s32, so); dma4(slot + 4096, ak, s0, s0, s32, s32, so + 4096); }
      dma4(slot + 8192, av, s0, s16, s0, s16, so); dma4(slot + 12288, av, s0, s16, s0, s16, so + 4096);
      if ((c & 3) == 3) lds_add(W_PAUSE, (((turn >> (8 * w)) >> 8) & 255) - ((turn >> (8 * w)) & 255));
      if (c & 3) { wait_vm<16>(); if ((c & 3) == 1) lds_wait_ge(W_AFULL, c - 1); lds_add(W_AFULL, 1); }
    }
    wait_vm<0>(); lds_add(W_AFULL, 1);
    served = true;
  };
  float floorv = -__builtin_inff(); unsigned force = 0;
  if (ROLE_B()) {
    NEWLANE();
    const unsigned s0 = src_lane<8>(lane), s16 = s0 ^ 16u, s32 = s0 ^ 32u;
    const unsigned ka = rd_rows(lane) + bring, va0 = rd_cols(lane) + bring + 8192, va1 = va0 ^ 8u;
    if (!late) { kv0(2, s0); kv0(3, s0); }
    wait_vm<8>();
    att_read_k(A, ka);
    const unsigned pmask = 0xff00u | (17u << w);
    auto rest = [&]() INL {
      if (lds_wait_clear_or(W_PAUSE, pmask, 16u << w) & (16u << w)) { serve(s0, s16, s32); lds_wait_clear_or(W_PAUSE, pmask, 0); }
    };
    unsigned pz = 0;
#pragma unroll 1
    for (unsigned c = 0; c < 16; ++c) {
      const unsigned so = ((c + 1 + cb) & 15) * 8192, so2 = so + 4096; unsigned ps = 0;
      const bool more = c != 15;
      att_chunk(A, floorv, force, va0, va1, ka, [&](auto mc) {
        constexpr int m = decltype(mc)::value;
        if constexpr (m == 44 || m == 30) pz = lds_peek(W_PAUSE);
        if constexpr (m == 1 || m == 35) ps = to_sgpr(pz);
        if constexpr (m == 4 || m == 38) { if (more) {
          if (__builtin_expect(ps & pmask, 0)) rest();
          if (m == 4) dma4(bring, sk, s0, s0, s32, s32, so); else dma4(bring + 8192, sv, s0, s16, s0, s16, so); } }
        if constexpr (m == 5 || m == 40) { if (more) { if (m == 5) dma4(bring + 4096, sk, s0, s0, s32, s32, so2); else dma4(bring + 12288, sv, s0, s16, s0, s16, so2); } }
        if constexpr (m == 16) { wait_vm<8>(); if (!more) wait_vm<0>(); }
        if constexpr (m == 43) wait_vm<8>();
      });
      floorv = 0.f; force = 1;
    }
  } else {
    NEWLANE();
    const unsigned kl = rd_rows(lane) + RX, vl = rd_cols(lane) + RX + 8192;
    att_read_k(A, kl + (RK0 - RX));
#pragma unroll 1
    for (unsigned c = 0; c < 16; ++c) {
      const unsigned need = c < 15 ? c + 2 : 16;
      const unsigned va0 = vl + (c % NSA) * 16384, va1 = va0 ^ 8u, ka = kl + ((c + 1) % NSA) * 16384;
      unsigned bz = 0, bs = 0;
      att_chunk(A, floorv, force, va0, va1, ka, [&](auto mc) {
        constexpr int m = decltype(mc)::value;
        if constexpr (m == 15) { if (__builtin_expect(c == 0, 0)) lds_wait_ge(W_AFULL, 1); }
        if constexpr (m == 36) bz = lds_peek(W_AFULL);
        if constexpr (m == 39) lds_add(W_AFREE, 1u << (8 * w));
        if constexpr (m == 40) bs = to_sgpr(bz);
        if constexpr (m == 42) { if (__builtin_expect(bs < need, 0)) lds_wait_ge(W_AFULL, need); }
      });
      floorv = 0.f; force = 1;
    }
    wait_until(t0, (pace >> 13) & 8191);
    lds_add(W_PAUSE, 256u);
    lds_wait_all_ge(W_AFREE, 17);
  }
  { const bool rb = ROLE_B();
    wait_lgkm0();
    NEWLANE();
    att_store_o(A, (rb ? bring : RX + w * 12288) + lane * 16);
    lds_add(rb ? W_OB : W_ABAR, rb ? 4u << (8 * w) : 1u); }

  if (ROLE_B()) {
    NEWLANE();
    if (!served) { lds_wait_set(W_PAUSE, 16u << w); const unsigned s0 = src_lane<8>(lane); serve(s0, s0 ^ 16u, s0 ^ 32u); }
    const u64 tok = get_token();
    lds_wait_ge(W_WDONE, 4);
    reduce_out(p.part + (size_t)((w >> 1) * 256 + b * 8) * 49152, p.flags + b * 128, p.out + (size_t)b * 98304 + (w >> 1) * 1024, j, w, tok, lane);
    if (j == 0 && w == 0 && lane == 0) __hip_atomic_store((gu64*)epoch, tok, __ATOMIC_RELAXED, __HIP_MEMORY_SCOPE_AGENT);
  } else {
    NEWLANE();
    gemm_cfg<10>(G, p.wo[0] + (size_t)j * 524288 + w * 256, ring, RX + lane * 16, b & 15, W_XB, lane);
    w_step<10>(ring, G.srd, G, 0, b & 15);
    lds_wait_ge(W_ABAR, 8);
    const u32x4 swb = make_srd(p.wo[1] + (size_t)j * 524288 + w * 256, (511u << 10) + 256u);
    gemm<10, 2, 12288>(acc, G, [&](int i) INL {
      if (i >= 0 && i < 8) {
        const unsigned so = w_off<10>(0, i >> 2, b & 3), so1 = w_odd<10>(so);
        if ((i & 3) == 0) { set_m0(ring + (i >> 2) * 4096); w_piece<0>(swb, G, so, so1); } else if ((i & 3) == 1) w_piece<1>(swb, G, so, so1);
        else if ((i & 3) == 2) w_piece<2>(swb, G, so, so1); else w_piece<3>(swb, G, so, so1); }
      if (i == 4) lds_add(W_ABAR, 1);
      if (i == 11) { lds_wait_ge(W_ABAR, 12); w_step<10>(RX + w * 8192, swb, G, 1, b & 3); lds_add(W_PAUSE, 0u - 256u); }
    });
    NEWLANE();
    char* const part = p.part + (size_t)((b * 8 + j) * 4 + w) * 12288;
    const unsigned sz0 = j == 2 * w ? 0u : 12288u, sz1 = j == 2 * w + 1 ? 0u : 12288u, pl = lane * 16;
    const __amdgpu_buffer_rsrc_t ra0 = __builtin_amdgcn_make_buffer_rsrc(part, 0, sz0, 0x00020000), ra1 = __builtin_amdgcn_make_buffer_rsrc(part, 0, sz1, 0x00020000);
    const __amdgpu_buffer_rsrc_t rb0 = __builtin_amdgcn_make_buffer_rsrc(part + (size_t)256 * 49152, 0, sz0, 0x00020000);
    const __amdgpu_buffer_rsrc_t rb1 = __builtin_amdgcn_make_buffer_rsrc(part + (size_t)256 * 49152, 0, sz1, 0x00020000);
    u32x4 pa[12];
#pragma unroll
    for (int f = 0; f < 12; ++f) { pa[f] = pack_raw(acc[2 * (f / 3)][f % 3], acc[2 * (f / 3) + 1][f % 3]); asm volatile("" : "+v"(pa[f])); }
#pragma unroll
    for (int e = 0; e < 2; ++e) if (j == 2 * w + e) {
#pragma unroll
      for (int f = 0; f < 6; ++f) *LDS_PTR(u32x4, ROWN + f * 1024 + pl) = pa[6 * e + f]; }
    auto put_a = [&](int f) INL { __builtin_amdgcn_raw_buffer_store_b128(pa[f], f < 6 ? ra0 : ra1, pl + f * 1024, 0, 16); };
    auto put_b = [&](int a) INL {
#pragma unroll
      for (int q = 0; q < 3; ++q) __builtin_amdgcn_raw_buffer_store_b128(pack_raw(acc[2 * a][q], acc[2 * a + 1][q]), a < 2 ? rb0 : rb1, pl + (a * 3 + q) * 1024, 0, 16);
      if (j == 2 * w + (a >> 1)) {
#pragma unroll
        for (int q = 0; q < 3; ++q) *LDS_PTR(u32x4, ROWN + 6144 + ((a & 1) * 3 + q) * 1024 + pl) = pack_raw(acc[2 * a][q], acc[2 * a + 1][q]); } };
    gemm_cfg<10>(G, p.wo[1] + (size_t)j * 524288 + w * 256, RX + w * 8192, RB + pl, b & 3, W_OB, lane);
    gemm<10, 4, 16384>(acc, G, [&](int i) INL {
      if (i == -1) lds_add(W_WDONE, 1);
      if (i >= 0 && i < 8) put_a(i);
      if (i == 8 || i == 9) { put_a(2 * i - 8); put_a(2 * i - 7); }
      if (i == 11 || i == 13 || i == 15) put_b((i - 11) / 2);
    });
    put_b(3);
    wait_lgkm0(); lds_add(W_SENT, 1);
    publish(p.flags + b * 128, j, w, get_token(), lane);
  }
}

extern "C" void kernel_launch(void* const* d_in, const int* in_sizes, int n_in, void* d_out, int out_size, void* d_ws, size_t ws_size, hipStream_t stream) {
  Params p; Pace pc;
  for (int br = 0; br < 2; ++br) {
    p.x[br] = (const char*)d_in[br * 5 + 0]; p.k[br] = (const char*)d_in[br * 5 + 1]; p.v[br] = (const char*)d_in[br * 5 + 2];
    p.wq[br] = (const char*)d_in[br * 5 + 3]; p.wo[br] = (const char*)d_in[br * 5 + 4];
  }
  p.part = (char*)d_ws; p.flags = (u64*)((char*)d_ws + (32u << 20)); p.out = (char*)d_out;
  const unsigned tq[4] = {1004, 2521, 1076, 1714}, dq[4] = {31, 76, 109, 120}, to[4] = {3104, 0, 3755, 4179}, dd[4] = {68, 0, 43, 68}, nb[4] = {0, 1, 1, 2};
  for (unsigned b = 0; b < 32; ++b) { const unsigned c = 2 * (b & 1) + (b >> 4), r = (b >> 1) & 7; pc.t[b] = (tq[c] + dq[c] * r) | (to[c] + dd[c] * r) << 13 | nb[c] << 26; }
  fused_k<<<256, 512, 0, stream>>>(p, pc);
}
```

```cpp
#include <hip/hip_runtime.h>
#include <stdint.h>
#include <utility>

typedef __bf16 bf16x8 __attribute__((ext_vector_type(8)));
typedef __bf16 bf16x4 __attribute__((ext_vector_type(4)));
typedef __bf16 bf16x2 __attribute__((ext_vector_type(2)));
typedef float f32x4 __attribute__((ext_vector_type(4)));
typedef float f32x2 __attribute__((ext_vector_type(2)));
typedef unsigned u32x4 __attribute__((ext_vector_type(4)));
typedef unsigned long long u64;
typedef __attribute__((address_space(1))) u64 gu64;
#define LDS_AS __attribute__((address_space(3)))
#define LDS_PTR(T, a) ((LDS_AS T*)(uintptr_t)(a))
#define DI __device__ __forceinline__
#define INL __attribute__((always_inline))
#define SBAR() __builtin_amdgcn_sched_barrier(0)

constexpr unsigned T_LATE = 165;
constexpr unsigned TURNS0 = 0x8f4f2f1fu, TURNS1 = 0x8d4e271bu, TURNS2 = 0x894c2613u;

constexpr unsigned RB = 0;
constexpr unsigned RX = 65536;
constexpr unsigned RA = 114688;
constexpr unsigned ROWN = RX + 32768;
constexpr unsigned RK0 = 147456;
constexpr unsigned RDUMMY = 155648;
constexpr unsigned RS = 163584, LDS_BYTES = 163840;
constexpr unsigned W_ABAR = RS, W_GO = RS + 16, W_BQP = RS + 32, W_PAUSE = RS + 48, W_OB = RS + 64, W_XB = RS + 96, W_TOKV = RS + 112, W_TOK = RS + 128  ,
                   W_SENT = RS + 144, W_AFULL = RS + 160, W_WDONE = RS + 176, W_AFREE = RS + 192, W_RED = RS + 208;
constexpr unsigned NSA = 4;
constexpr float QSCALE = 0.08838834764831845f * 1.4426950408889634f;
constexpr float BIAS = 7.0f;

struct Params {
  const char* x[2]; const char* k[2]; const char* v[2]; const char* wq[2]; const char* wo[2];
  char* part; u64* flags; char* out;
};
struct Pace { unsigned t[32]; };

DI u32x4 make_srd(const char* p, unsigned bytes) { u32x4 s = {(unsigned)(uintptr_t)p, (unsigned)((uintptr_t)p >> 32), bytes, 0x00020000u}; return s; }
template <int N> DI void wait_vm() { asm volatile("s_waitcnt vmcnt(%0)" :: "i"(N) : "memory"); }
DI void wait_lgkm0() { asm volatile("s_waitcnt lgkmcnt(0)" ::: "memory"); }
template <int N> DI void wait_lgkm() { asm volatile("" ::: "memory"); __builtin_amdgcn_s_waitcnt(0xC07F | (N << 8)); }
DI void set_m0(unsigned dst) { asm volatile("s_mov_b32 m0, %0\n\ts_nop 0" :: "s"(dst) : "memory"); }
template <int OFF, int NT> DI void dma(u32x4 srd, unsigned v, unsigned so) {
  if constexpr (NT) asm volatile("buffer_load_dwordx4 %0, %1, %3 offen offset:%2 nt lds" :: "v"(v), "s"(srd), "i"(OFF), "s"(so) : "memory");
  else asm volatile("buffer_load_dwordx4 %0, %1, %3 offen offset:%2 lds" :: "v"(v), "s"(srd), "i"(OFF), "s"(so) : "memory");
}
DI void dma4(unsigned dst, u32x4 srd, unsigned v0, unsigned v1, unsigned v2, unsigned v3, unsigned so) {
  set_m0(dst); dma<0, 1>(srd, v0, so); dma<1024, 1>(srd, v1, so); dma<2048, 1>(srd, v2, so); dma<3072, 1>(srd, v3, so);
}
DI bf16x4 tr_read(unsigned a) { return __builtin_amdgcn_ds_read_tr16_b64_v4bf16(LDS_PTR(bf16x4, a)); }
DI bf16x8 ld128(unsigned a) { return *LDS_PTR(bf16x8, a); }
DI bf16x8 cat(bf16x4 a, bf16x4 b) { return __builtin_shufflevector(a, b, 0, 1, 2, 3, 4, 5, 6, 7); }
DI f32x4 mfma(bf16x8 a, bf16x8 b, f32x4 c) { return __builtin_amdgcn_mfma_f32_16x16x32_bf16(a, b, c, 0, 0, 0); }
DI unsigned cvtpk(float lo, float hi) { f32x2 v = {lo, hi}; bf16x2 b = __builtin_convertvector(v, bf16x2); return __builtin_bit_cast(unsigned, b); }
DI u32x4 pack_raw(f32x4 t0, f32x4 t1) { return u32x4{cvtpk(t0[0], t0[1]), cvtpk(t0[2], t0[3]), cvtpk(t1[0], t1[1]), cvtpk(t1[2], t1[3])}; }
DI u32x4 pack_pair(f32x4 t0, f32x4 t1, bool odd) { const u32x4 r = pack_raw(t0, t1); return u32x4{odd ? r[2] : r[0], odd ? r[3] : r[1], odd ? r[0] : r[2], odd ? r[1] : r[3]}; }
DI unsigned fresh_lane() { unsigned l; asm volatile("v_mbcnt_lo_u32_b32 %0, -1, 0\n\tv_mbcnt_hi_u32_b32 %0, -1, %0" : "=v"(l)); return l; }
DI float max_groups(float d) {
  auto r = __builtin_amdgcn_permlane16_swap(__float_as_uint(d), __float_as_uint(d), false, false);
  d = fmaxf(__uint_as_float(r[0]), __uint_as_float(r[1]));
  auto u = __builtin_amdgcn_permlane32_swap(__float_as_uint(d), __float_as_uint(d), false, false);
  return fmaxf(__uint_as_float(u[0]), __uint_as_float(u[1]));
}

DI void lds_add(unsigned addr, unsigned val) {
  asm volatile("s_mov_b64 exec, 1\n\tds_add_u32 %0, %1\n\ts_mov_b64 exec, -1" :: "v"(addr), "v"(val) : "memory");
}
DI unsigned lds_add_rtn(unsigned addr, unsigned val) {
  unsigned v, r;
  asm volatile("s_mov_b64 exec, 1\n\tds_add_rtn_u32 %0, %2, %3\n\ts_waitcnt lgkmcnt(0)\n\tv_readfirstlane_b32 %1, %0\n\ts_mov_b64 exec, -1\n\ts_nop 0\n\tv_mov_b32 %0, %1\n\ts_nop 0"
               : "=&v"(v), "=&s"(r) : "v"(addr), "v"(val) : "memory");
  return v;
}
#define LDS_SPIN(TEST, SLEEP) "s_mov_b64 exec, 1\n1:\n\tds_read_b32 %0, %3\n\ts_waitcnt lgkmcnt(0)\n\tv_readfirstlane_b32 %1, %0\n\ts_nop 0\n\t" TEST "s_sleep " SLEEP "\n\ts_sub_u32 %2, %2, 1\n\ts_cbranch_scc0 1b\n2:\n\ts_mov_b64 exec, -1"
DI void lds_wait_ge(unsigned addr, unsigned target) {
  unsigned v, sv, cnt = 1u << 20;
  asm volatile(LDS_SPIN("s_cmp_ge_u32 %1, %4\n\ts_cbranch_scc1 2f\n\t", "2") : "=&v"(v), "=&s"(sv), "+&s"(cnt) : "v"(addr), "s"(target) : "memory", "scc");
}
DI void lds_wait_all_ge(unsigned addr, unsigned n) {
  unsigned v, sv, cnt = 1u << 20;
  asm volatile(LDS_SPIN("s_add_u32 %1, %1, %4\n\ts_andn2_b32 %1, 0x80808080, %1\n\ts_cbranch_scc0 2f\n\t", "2")
               : "=&v"(v), "=&s"(sv), "+&s"(cnt) : "v"(addr), "s"((128u - n) * 0x01010101u) : "memory", "scc");
}
DI void lds_wait_set(unsigned addr, unsigned set) {
  unsigned v, sv, cnt = 1u << 20;
  asm volatile(LDS_SPIN("s_and_b32 %1, %1, %4\n\ts_cbranch_scc1 2f\n\t", "4") : "=&v"(v), "=&s"(sv), "+&s"(cnt) : "v"(addr), "s"(set) : "memory", "scc");
}
DI unsigned lds_wait_clear_or(unsigned addr, unsigned mask, unsigned set) {
  unsigned v, sv, cnt = 1u << 20;
  asm volatile(LDS_SPIN("s_and_b32 %1, %1, %4\n\ts_cbranch_scc0 2f\n\ts_and_b32 %1, %1, %5\n\ts_cbranch_scc1 2f\n\t", "4")
               : "=&v"(v), "=&s"(sv), "+&s"(cnt) : "v"(addr), "s"(mask), "s"(set) : "memory", "scc");
  return __builtin_amdgcn_readfirstlane(v);
}
DI unsigned lds_peek(unsigned addr) { return *(volatile LDS_AS unsigned*)(uintptr_t)addr; }
DI unsigned to_sgpr(unsigned v) { unsigned r = __builtin_amdgcn_readfirstlane(v); asm volatile("" : "+s"(r)); return r; }
DI unsigned now_ticks() { return (unsigned)__builtin_amdgcn_s_memrealtime(); }
DI void wait_until(unsigned t0, unsigned ticks) {
#pragma unroll 1
  while (now_ticks() - t0 + 90 < ticks) __builtin_amdgcn_s_sleep(24);
#pragma unroll 1
  while (now_ticks() - t0 < ticks) __builtin_amdgcn_s_sleep(4);
}

template <int SH> DI unsigned src_lane(unsigned lane) { return (((lane >> 2) & 3) << SH) + (lane >> 4) * 64 + (lane & 3) * 16; }
DI unsigned rd_rows(unsigned lane) { const unsigned g = lane >> 4, m = lane & 15; return (m >> 2) * 1024 + (m & 3) * 64 + ((g ^ ((m >> 2) & 2)) << 4); }
DI unsigned rd_cols(unsigned lane) { const unsigned g = lane >> 4, q = (lane >> 2) & 3, pp = lane & 3; return g * 1024 + q * 64 + ((pp ^ (g & 1)) << 4) + 8 * (pp & 1); }

struct Frag { bf16x4 lo[8], hi[8]; bf16x8 b[3]; };
struct GemmCfg { u32x4 srd; unsigned wv[4]; unsigned ring, ga0, ga1, bl, rot, gv; };
template <int SH> DI void gemm_cfg(GemmCfg& G, const char* w, unsigned ring, unsigned bl, unsigned rot, unsigned gate, unsigned lane) {
  constexpr unsigned U = (8u << SH) - 1024u;
  const unsigned w0 = src_lane<SH>(lane);
  G.srd = make_srd(w, (511u << SH) + 256u);
  G.wv[0] = w0; G.wv[1] = (w0 ^ 16u) + U; G.wv[2] = w0 + 2 * U; G.wv[3] = (w0 ^ 16u) + 3 * U;
  G.ring = ring; G.ga0 = rd_cols(lane) + ring; G.ga1 = G.ga0 ^ 8u; G.bl = bl; G.rot = rot; G.gv = gate; asm volatile("" : "+v"(G.gv));
}
template <int SH> DI unsigned w_off(unsigned s, unsigned t, unsigned rot) { return (((s ^ rot) << 5) + 4u * t) << SH; }
template <int SH> DI unsigned w_odd(unsigned so) { return SH == 10 ? so ^ (4u << SH) : so; }
template <int U> DI void w_piece(u32x4 srd, const GemmCfg& G, unsigned so, unsigned so1) { dma<1024 * U, 0>(srd, G.wv[U], (U & 1) ? so1 : so); }
template <int SH> DI void w_step(unsigned dst, u32x4 srd, const GemmCfg& G, unsigned s, unsigned rot) {
#pragma unroll
  for (int t = 0; t < 2; ++t) { const unsigned so = w_off<SH>(s, t, rot), so1 = w_odd<SH>(so);
    set_m0(dst + t * 4096); w_piece<0>(srd, G, so, so1); w_piece<1>(srd, G, so, so1); w_piece<2>(srd, G, so, so1); w_piece<3>(srd, G, so, so1); }
}
template <int SH, int NS, int BK, int MODE, int PAR, class Tail>
DI void gstep(f32x4 (&acc)[8][3], const Frag& c, Frag& n, const GemmCfg& G, unsigned s, Tail tail) {
  constexpr unsigned BS = SH == 13 ? 256 : 1024, SL = NS == 4 && PAR == 0 ? RA - RX : 0;
  u32x4 srd = G.srd;
  if (NS == 4 && MODE == 1 && PAR == 0) srd[2] = s < 14 ? srd[2] : 0u;
#define MQ(k) ((((k) / 3) & 1) ? 2 - (k) % 3 : (k) % 3)
#define M(k) do { if (MODE != 0) acc[4 * t + (k) / 3][MQ(k)] = mfma(cat(c.lo[4 * t + (k) / 3], c.hi[4 * t + (k) / 3]), c.b[MQ(k)], acc[4 * t + (k) / 3][MQ(k)]); SBAR(); } while (0)
#define RD(i) d[i] = tr_read((((i) & 1) ? G.ga1 : G.ga0) + SL + t * 4096 + ((i) >> 1) * 256)
#define PC(u) do { if (MODE != 2) w_piece<u>(srd, G, so, so1); else { if (u == 0 && t == 0) wait_vm<4 * NS - 8>(); tail(4 * t + u); } } while (0)
  const unsigned se = s ^ G.rot, gbit = 4u << (8 * (se >> 2));
  unsigned xb = 0, gz = 0, gs = 0;
#pragma unroll
  for (int t = 0; t < 2; ++t) {
    bf16x4* d = t ? n.hi : n.lo;
    if (MODE == 2 && t == 0) tail(-1);
    if (MODE != 2 || t == 0) wait_vm<4 * NS - 4>();
    SBAR();
    M(0); RD(0); RD(1); RD(2); SBAR();
    M(1); RD(3); RD(4); RD(5); SBAR();
    M(2); RD(6); RD(7); SBAR();
    M(3);
    if (t == 0 && PAR == 0) gz = lds_peek(G.gv);
    if (t == 1) { if (PAR == 0) { if (__builtin_expect(!(gs & gbit), 0)) lds_wait_set(G.gv, gbit); } n.b[0] = ld128(xb); n.b[1] = ld128(xb + 4096); }
    SBAR();
    const unsigned so = w_off<SH>((s + NS / 2) & 15u, t, G.rot), so1 = w_odd<SH>(so);
    M(4); if (t == 1) n.b[2] = ld128(xb + 8192); SBAR();
    if (MODE != 2) asm volatile("" :: "s"(so), "s"(so1));
    M(5); SBAR();
    M(6); if (MODE == 0) set_m0(G.ring + SL + t * 4096); if (MODE == 1) asm volatile("s_mov_b32 m0, %0" :: "s"(G.ring + SL + t * 4096) : "memory"); SBAR();
    M(7); wait_lgkm<0>(); SBAR(); PC(0); SBAR();
    M(8); PC(1); if (t == 0 && PAR == 0) gs = to_sgpr(gz); SBAR();
    M(9); PC(2); if (t == 0) { xb = G.bl + ((se & 3) * BS + (se >> 2) * BK); asm volatile("" :: "v"(xb)); } SBAR();
    M(10); PC(3); SBAR();
    M(11);
  }
#undef PC
#undef MQ
#undef M
#undef RD
}
template <int SH, int NS, int BK, class Tail>
DI void gemm(f32x4 (&acc)[8][3], const GemmCfg& G, Tail tail) {
#pragma unroll
  for (int a = 0; a < 8; ++a)
#pragma unroll
    for (int q = 0; q < 3; ++q) acc[a][q] = f32x4{0.f, 0.f, 0.f, 0.f};
  Frag X, Y;
  gstep<SH, NS, BK, 0, 0>(acc, Y, X, G, 0, tail);
#pragma unroll 1
  for (unsigned s = 1; s < 15; s += 2) {
    gstep<SH, NS, BK, 1, 1>(acc, X, Y, G, s, tail);
    gstep<SH, NS, BK, 1, 0>(acc, Y, X, G, s + 1, tail);
  }
  gstep<SH, NS, BK, 2, 1>(acc, X, Y, G, 15, tail);
#pragma unroll
  for (int i = 0; i < 8; ++i) {
#pragma unroll
    for (int r = 0; r < 3; ++r) { const int q = (i & 1) ? 2 - r : r; acc[i][q] = mfma(cat(Y.lo[i], Y.hi[i]), Y.b[q], acc[i][q]); SBAR(); }
    SBAR(); tail(8 + i); SBAR();
  }
}

template <class F, int... Ms> DI void for_seq(F& f, std::integer_sequence<int, Ms...>) { (f(std::integral_constant<int, Ms>{}), ...); }
struct Att { bf16x8 q[4][3]; f32x4 o[8][3]; f32x4 l[3]; f32x4 nm[3]; bf16x8 kf[8]; u32x4 ones; };

template <class Hook>
DI void att_chunk(Att& A, float floorv, unsigned force, unsigned va0, unsigned va1, unsigned ka, Hook hook) {
  f32x4 s[3][2]; u32x4 pw[3]; bf16x4 vlo[8], vhi[8]; float ex[3][8]; unsigned tt[3]; u64 big[3];
#define IC(n) std::integral_constant<int, (n)>{}
  auto mm = [&](auto mc) {
    constexpr int m = decltype(mc)::value;
    if constexpr (m < 24) { constexpr int q = m >> 3, kt = (m >> 2) & 1, i = m & 3; s[q][kt] = mfma(A.kf[4 * kt + i], A.q[i][q], i == 0 ? A.nm[q] : s[q][kt]); }
    else { constexpr int q = (m - 24) / 9, i = (m - 24) % 9;
      if constexpr (i == 0) A.l[q] = mfma(__builtin_bit_cast(bf16x8, A.ones), __builtin_bit_cast(bf16x8, pw[q]), A.l[q]);
      else A.o[i - 1][q] = mfma(cat(vlo[i - 1], vhi[i - 1]), __builtin_bit_cast(bf16x8, pw[q]), A.o[i - 1][q]); }
    SBAR();
  };
  auto E = [&](auto qc, auto ic) { constexpr int q = decltype(qc)::value, i = decltype(ic)::value;
    ex[q][i] = __builtin_amdgcn_exp2f(s[q][i >> 2][i & 3]); asm volatile("" :: "v"(ex[q][i])); SBAR(); };
  auto C = [&](auto qc, auto ic) { constexpr int q = decltype(qc)::value, i = decltype(ic)::value;
    const unsigned wd = cvtpk(ex[q][2 * i], ex[q][2 * i + 1]); asm volatile("" :: "v"(wd)); pw[q][i] = wd; SBAR(); };
  auto T1 = [&](auto qc) { constexpr int q = decltype(qc)::value; tt[q] = pw[q][0] | pw[q][1] | pw[q][2]; asm volatile("" : "+v"(tt[q])); SBAR(); };
  auto T2 = [&](auto qc) { constexpr int q = decltype(qc)::value;
    big[q] = __ballot(((tt[q] | pw[q][3]) & 0x40004000u) >= force); asm volatile("" : "+s"(big[q])); SBAR(); };
  auto BR = [&](auto qc) { constexpr int q = decltype(qc)::value;
    if (__builtin_expect(big[q] != 0, 0)) {
      float d = fmaxf(fmaxf(fmaxf(s[q][0][0], s[q][0][1]), fmaxf(s[q][0][2], s[q][0][3])), fmaxf(fmaxf(s[q][1][0], s[q][1][1]), fmaxf(s[q][1][2], s[q][1][3])));
      d = fmaxf(max_groups(d) + BIAS, floorv);
      const float f = __builtin_amdgcn_exp2f(fminf(-d, 0.f));
#pragma unroll
      for (int r = 0; r < 4; ++r) { A.nm[q][r] -= d; A.l[q][r] *= f;
        pw[q][r] = cvtpk(__builtin_amdgcn_exp2f(s[q][r >> 1][2 * (r & 1)] - d), __builtin_amdgcn_exp2f(s[q][r >> 1][2 * (r & 1) + 1] - d)); }
      if (force) {
#pragma unroll
        for (int i = 0; i < 8; ++i) A.o[i][q] *= f; }
    }
    SBAR(); };
  auto gap = [&](auto mc) {
    constexpr int m = decltype(mc)::value;
    if constexpr (m == 0) wait_lgkm<4>();
    if constexpr (m == 4 || m == 29) wait_lgkm<0>();
    if constexpr (m == 25) wait_lgkm<8>();
    SBAR();
    mm(mc);
    hook(mc);
    SBAR();
    if constexpr (m >= 16 && m < 24) { vlo[m - 16] = tr_read((((m - 16) & 1) ? va1 : va0) + ((m - 16) >> 1) * 256);
                                       vhi[m - 16] = tr_read((((m - 16) & 1) ? va1 : va0) + ((m - 16) >> 1) * 256 + 4096); }
    if constexpr (m >= 43) A.kf[m - 43] = ld128(ka + ((m - 43) >> 2) * 4096 + ((m - 43) & 3) * 256);
    SBAR();
    if constexpr (m >= 6 && m < 14) E(IC(0), IC(m - 6));
    if constexpr (m == 14 || m == 15) E(IC(1), IC(m - 14));
    if constexpr (m == 20) E(IC(1), IC(2));
    if constexpr (m >= 24 && m < 29) E(IC(1), IC(m - 21));
    if constexpr (m >= 29 && m < 37) E(IC(2), IC(m - 29));
    if constexpr (m == 8 || m == 10 || m == 12 || m == 14) C(IC(0), IC((m - 8) / 2));
    if constexpr (m == 13) T1(IC(0));
    if constexpr (m == 15) T2(IC(0));
    if constexpr (m == 18) BR(IC(0));
    if constexpr (m == 17) C(IC(1), IC(0));
    if constexpr (m == 25 || m == 27 || m == 29) C(IC(1), IC((m - 23) / 2));
    if constexpr (m == 28) T1(IC(1));
    if constexpr (m == 30) T2(IC(1));
    if constexpr (m == 32) BR(IC(1));
    if constexpr (m == 31 || m == 33 || m == 35 || m == 37) C(IC(2), IC((m - 31) / 2));
    if constexpr (m == 37) T1(IC(2));
    if constexpr (m == 38) T2(IC(2));
    if constexpr (m == 41) BR(IC(2));
    SBAR();
  };
  for_seq(gap, std::make_integer_sequence<int, 51>{});
#undef IC
}
DI void att_init(Att& A, f32x4 (&acc)[8][3], unsigned lane) {
  const bool odd = (lane >> 4) & 1;
#pragma unroll
  for (int a = 0; a < 4; ++a)
#pragma unroll
    for (int q = 0; q < 3; ++q) {
      A.q[a][q] = __builtin_bit_cast(bf16x8, pack_pair(acc[2 * a][q] * QSCALE, acc[2 * a + 1][q] * QSCALE, odd));
      A.o[2 * a][q] = f32x4{0.f, 0.f, 0.f, 0.f}; A.o[2 * a + 1][q] = f32x4{0.f, 0.f, 0.f, 0.f};
    }
#pragma unroll
  for (int q = 0; q < 3; ++q) { A.l[q] = f32x4{0.f, 0.f, 0.f, 0.f}; A.nm[q] = f32x4{0.f, 0.f, 0.f, 0.f}; }
  A.ones = u32x4{0x3f803f80u, 0x3f803f80u, 0x3f803f80u, 0x3f803f80u}; asm volatile("" : "+v"(A.ones));
}
DI void att_read_k(Att& A, unsigned ka) {
#pragma unroll
  for (int i = 0; i < 8; ++i) A.kf[i] = ld128(ka + (i >> 2) * 4096 + (i & 3) * 256);
  SBAR(); wait_lgkm<0>(); SBAR();
}
DI void att_store_o(Att& A, unsigned obuf_lane) {
#pragma unroll
  for (int q = 0; q < 3; ++q) {
    const float inv = __builtin_amdgcn_rcpf(A.l[q][0]);
#pragma unroll
    for (int a = 0; a < 4; ++a) *LDS_PTR(u32x4, obuf_lane + q * 4096 + a * 1024) = pack_raw(A.o[2 * a][q] * inv, A.o[2 * a + 1][q] * inv);
  }
  wait_lgkm0();
}

DI void publish(u64* fl  , unsigned j, unsigned w, u64 tok, unsigned lane) {
  wait_vm<0>();
  if (lane < 2 && 2 * w + lane != j) __hip_atomic_store((gu64*)(fl + (2 * w + lane) * 16 + j), tok, __ATOMIC_RELAXED, __HIP_MEMORY_SCOPE_AGENT);
}
DI void reduce_out(const char* part  , u64* fl  , char* out  , unsigned j, unsigned w, u64 tok, unsigned lane) {
  const u32x4 srd = make_srd(part + (j >> 1) * 12288 + ((j & 1) * 2 + (w & 1)) * 3072, 7 * 49152 + 3072);
  gu64* f = (gu64*)(fl + j * 16 + ((lane & 7) ^ j));
  const unsigned vo = lane * 16, ol = ROWN + w * 3072 + vo;
  u32x4 v[8][3]; f32x2 ps[4][12];
#pragma unroll
  for (int i = 1; i < 8; ++i) asm volatile("" : "=v"(v[i][0]), "=v"(v[i][1]), "=v"(v[i][2]));
#pragma unroll
  for (int i = 0; i < 4; ++i)
#pragma unroll
    for (int e = 0; e < 12; ++e) asm volatile("" : "=v"(ps[i][e]));
  u64 x = __hip_atomic_load(f, __ATOMIC_RELAXED, __HIP_MEMORY_SCOPE_AGENT);
  lds_wait_ge(W_SENT, 4);
  unsigned asked = 1, summed = 0;
#pragma unroll 1
  for (unsigned spin = 0; spin < (1u << 20); ++spin) {
    asm volatile("s_waitcnt vmcnt(0)" : "+v"(x) :: "memory");
    const unsigned landed = asked, nw = (unsigned)__ballot(x == tok) & 255u & ~asked;
#pragma unroll
    for (int i = 1; i < 8; ++i)
      if (nw & (1u << i))
        asm volatile("buffer_load_dwordx4 %0, %3, %4, %5 offen sc1\n\tbuffer_load_dwordx4 %1, %3, %4, %5 offen offset:1024 sc1\n\tbuffer_load_dwordx4 %2, %3, %4, %5 offen offset:2048 sc1"
                     : "+v"(v[i][0]), "+v"(v[i][1]), "+v"(v[i][2]) : "v"(vo), "s"(srd), "s"((i ^ j) * 49152u) : "memory");
    asked |= nw;
    if (asked != 255u) x = __hip_atomic_load(f, __ATOMIC_RELAXED, __HIP_MEMORY_SCOPE_AGENT);
    const unsigned pairs = landed & (landed >> 1) & 0x55u & ~summed;
#pragma unroll
    for (int i = 3; i >= 0; --i)
      if (pairs & (1u << (2 * i))) {
        if (i == 0) { v[0][0] = *LDS_PTR(u32x4, ol); v[0][1] = *LDS_PTR(u32x4, ol + 1024); v[0][2] = *LDS_PTR(u32x4, ol + 2048); }
        asm volatile("" : "+v"(v[2 * i][0]), "+v"(v[2 * i][1]), "+v"(v[2 * i][2]), "+v"(v[2 * i + 1][0]), "+v"(v[2 * i + 1][1]), "+v"(v[2 * i + 1][2]));
#pragma unroll
        for (int e = 0; e < 12; ++e) { const unsigned a = v[2 * i][e >> 2][e & 3], c = v[2 * i + 1][e >> 2][e & 3];
          ps[i][e] = f32x2{__uint_as_float(a << 16), __uint_as_float(a & 0xffff0000u)} + f32x2{__uint_as_float(c << 16), __uint_as_float(c & 0xffff0000u)}; }
      }
    summed |= pairs;
    if (landed == 255u) break;
  }
  wait_vm<0>();
  const __amdgpu_buffer_rsrc_t ro = __builtin_amdgcn_make_buffer_rsrc(out + j * 128 + (w & 1) * 64, 0, 47 * 2048 + 64, 0x00020000);
#pragma unroll
  for (int q = 0; q < 3; ++q) {
    f32x2 s[4];
#pragma unroll
    for (int e = 0; e < 4; ++e) s[e] = (ps[0][4 * q + e] + ps[1][4 * q + e]) + (ps[2][4 * q + e] + ps[3][4 * q + e]);
    __builtin_amdgcn_raw_buffer_store_b128(pack_pair(f32x4{s[0][0], s[0][1], s[1][0], s[1][1]}, f32x4{s[2][0], s[2][1], s[3][0], s[3][1]}, (lane >> 4) & 1),
                                           ro, (q * 16 + (lane & 15)) * 2048 + (lane >> 4) * 16, 0, 16);
  }
  if (lds_add_rtn(W_RED, 1) == 3 && lane > 0 && lane < 8) __hip_atomic_store(f, ~tok, __ATOMIC_RELAXED, __HIP_MEMORY_SCOPE_AGENT);
}
DI u64 get_token() {
  lds_wait_ge(W_TOKV, 1);
  const unsigned lo = lds_peek(W_TOK), hi = lds_peek(W_TOK + 4);
  return ((u64)(unsigned)__builtin_amdgcn_readfirstlane(hi) << 32) | (unsigned)__builtin_amdgcn_readfirstlane(lo);
}
template <int N, int SH> DI void touch_rows(const char* p0, unsigned lane) {
  const u32x4 sr = make_srd(p0, ((N - 1) << SH) + 1024);
#pragma unroll
  for (int i = 0; i < N; ++i) dma<0, 0>(sr, lane * 16, (unsigned)i << SH);
}
template <int NT> DI void load_x(const char* x, unsigned w, unsigned r) {
  asm volatile("" : "+s"(w));
  const u32x4 sx = make_srd(x, 49152);
  const unsigned v = src_lane<10>(fresh_lane());
  const unsigned v0 = v ^ (((3 * w) & 2) << 4), v1 = (v ^ (((3 * w + 1) & 2) << 4)) + 3072, v2 = (v ^ (((3 * w + 2) & 2) << 4)) + 6144;
#pragma unroll
  for (int i = 0; i < 4; ++i) { const unsigned kq = i ^ r, so = kq * 256 + w * 12288;
    set_m0(RX + kq * 12288 + w * 3072); dma<0, NT>(sx, v0, so); dma<1024, NT>(sx, v1, so); dma<2048, NT>(sx, v2, so); }
}

__global__ __launch_bounds__(512, 2) void fused_k(Params p, Pace pc) {
  __shared__ __attribute__((aligned(1024))) char smem[LDS_BYTES];
  asm volatile("" :: "v"((LDS_AS char*)smem));
  const unsigned tid = threadIdx.x;
  unsigned lane;
#define NEWLANE() (lane = fresh_lane())
  const unsigned wid = __builtin_amdgcn_readfirstlane(tid >> 6), w = wid & 3;
  const unsigned j = blockIdx.x & 7, b = blockIdx.x >> 3, h = 4 * j + w, rot = b >> 1;
  const bool late = b & 1;
  const unsigned t0 = now_ticks(), pace = pc.t[b];
  asm volatile("" :: "s"(p.x[0]), "s"(p.x[1]), "s"(p.k[0]), "s"(p.k[1]), "s"(p.v[0]), "s"(p.v[1]), "s"(p.wq[0]), "s"(p.wq[1]), "s"(p.wo[0]), "s"(p.wo[1]),
               "s"(p.part), "s"(p.flags), "s"(p.out), "s"(t0), "s"(pace));
  if (tid < 64) *LDS_PTR(unsigned, RS + tid * 4) = 0;
  wait_lgkm0();
  __builtin_amdgcn_s_barrier();
  GemmCfg G;
  const unsigned ring = RA + w * 8192, bring = RB + w * 16384;
  if (!late) {
    if (wid < 4) { gemm_cfg<13>(G, p.wq[1] + h * 256, ring, 0, rot, 0, NEWLANE()); w_step<13>(ring, G.srd, G, 0, rot); }
    else load_x<0>(p.x[1] + (size_t)b * 49152, w, rot >> 2);
  }
  unsigned role = wid;
#define ROLE_B() ({ asm volatile("" : "+s"(role)); role < 4; })
  f32x4 acc[8][3];
  Att A;
#pragma unroll
  for (int i = 0; i < 8; ++i) asm volatile("" : "=v"(A.kf[i]));
  u64* const epoch = p.flags + 256 * 16 + b * 16;
  const unsigned turn = (pace >> 26) == 0 ? TURNS0 : (pace >> 26) == 1 ? TURNS1 : TURNS2;
  const unsigned cb = h, ca = b * 8 + j;
  const u32x4 sk = make_srd(p.k[1] + (size_t)(b * 32 + h) * 131072, 131072), sv = make_srd(p.v[1] + (size_t)(b * 32 + h) * 131072, 131072);
  auto kv0 = [&](int i, unsigned s0) INL {
    const unsigned so = (cb & 15) * 8192 + (i & 1) * 4096;
    if (i < 2) dma4(bring + i * 4096, sk, s0, s0, s0 ^ 32u, s0 ^ 32u, so); else dma4(bring + i * 4096, sv, s0, s0 ^ 16u, s0, s0 ^ 16u, so); };

  if (ROLE_B()) {
    if (late) {
      wait_until(t0, T_LATE);
      const unsigned s0 = src_lane<8>(NEWLANE());
      kv0(0, s0); kv0(1, s0); kv0(2, s0); kv0(3, s0);
      wait_vm<8>();
      att_read_k(A, rd_rows(lane) + bring);
      const unsigned so = ((cb + 1) & 15) * 8192;
      dma4(bring, sk, s0, s0, s0 ^ 32u, s0 ^ 32u, so); dma4(bring + 4096, sk, s0, s0, s0 ^ 32u, s0 ^ 32u, so + 4096);
      lds_wait_ge(W_GO, 4);
    }
  } else {
    NEWLANE();
    if (late) {
      wait_until(t0, T_LATE);
      __builtin_amdgcn_s_setprio(1);
      load_x<0>(p.x[1] + (size_t)b * 49152, w, 0);
      const unsigned r0 = (rot * 4 + w) * 8;
      set_m0(RDUMMY);
      touch_rows<3, 10>(p.x[0] + (size_t)(b - 1) * 49152 + (j * 12 + w * 3) * 1024, lane);
      touch_rows<8, 13>(p.wq[0] + (size_t)r0 * 8192 + j * 1024, lane);
      touch_rows<8, 10>(p.wo[0] + (size_t)(512 * j + r0) * 1024, lane);
      touch_rows<8, 10>(p.wo[1] + (size_t)(512 * j + r0) * 1024, lane);
      lds_add(W_GO, 1);
      wait_vm<27>(); lds_add(W_XB, 0x01010101u);
    } else {
      const unsigned r = rot >> 2;
      wait_vm<9>(); lds_add(W_XB, 1u << (8 * r));
      wait_vm<6>(); lds_add(W_XB, 1u << (8 * (r ^ 1)));
      wait_vm<3>(); lds_add(W_XB, 1u << (8 * (r ^ 2)));
      wait_vm<0>(); lds_add(W_XB, 1u << (8 * (r ^ 3)));
    }
    __builtin_amdgcn_s_setprio(1);
    lds_wait_ge(W_BQP, 4);
    load_x<1>(p.x[0] + (size_t)b * 49152, w, 0);
    { const unsigned s0 = src_lane<8>(lane) ^ ((w & 1) << 5), so = (ca & 15) * 8192 + w * 2048;
      const u32x4 ak = make_srd(p.k[0] + (size_t)(b * 8 + j) * 131072, 131072);
      set_m0(RK0 + w * 2048); dma<0, 1>(ak, s0, so); dma<1024, 1>(ak, s0, so); }
    if (w == 0) {
      const u64 tk = __hip_atomic_load((gu64*)epoch, __ATOMIC_RELAXED, __HIP_MEMORY_SCOPE_AGENT) + 0x9E3779B97F4A7C15ull;
      if (lane == 0) *LDS_PTR(u64, W_TOK) = tk;
      wait_lgkm0();
      lds_add(W_TOKV, 1);
    }
    wait_vm<0>();
    lds_add(W_ABAR, 1);
    wait_until(t0, pace & 8191);
    lds_add(W_PAUSE, w == 0 ? 256u + (turn & 255) : 256u);
    lds_wait_ge(W_ABAR, 4);
  }

  { const bool rb = ROLE_B(), early = rb && !late;
    NEWLANE();
    gemm_cfg<13>(G, (rb ? p.wq[1] : p.wq[0]) + h * 256, ring, rd_rows(lane) + RX, rot, W_XB, lane);
    if (!early) w_step<13>(ring, G.srd, G, 0, rot);
    const unsigned s0 = src_lane<8>(lane);
    gemm<13, 2, 12288>(acc, G, [&](int i) INL {
      if (i == -1) { if (!rb) lds_add(W_PAUSE, 0u - 256u); }
      if (i == 0 || i == 3) { if (early) kv0(i / 3, s0); }
      if (i == 4) lds_add(rb ? W_BQP : W_AFREE, rb ? 1u : 1u << (8 * w));
    });
    att_init(A, acc, NEWLANE());
    if (early) { const unsigned s1 = src_lane<8>(lane); kv0(2, s1); kv0(3, s1); wait_vm<8>(); att_read_k(A, rd_rows(lane) + bring); } }

  bool served = false;
  auto serve = [&](unsigned s0, unsigned s16, unsigned s32) INL {
    const u32x4 ak = make_srd(p.k[0] + (size_t)(b * 8 + j) * 131072, 131072), av = make_srd(p.v[0] + (size_t)(b * 8 + j) * 131072, 131072);
#pragma unroll 1
    for (unsigned c = 4 * w; c < 4 * w + 4; ++c) {
      lds_wait_all_ge(W_AFREE, c < NSA ? 1 : c - NSA + 2);
      const unsigned so = ((c + ca) & 15) * 8192, slot = RX + (c % NSA) * 16384;
      if (c) { dma4(slot, ak, s0, s0, s32, s32, so); dma4(slot + 4096, ak, s0, s0, s32, s32, so + 4096); }
      dma4(slot + 8192, av, s0, s16, s0, s16, so); dma4(slot + 12288, av, s0, s16, s0, s16, so + 4096);
      if ((c & 3) == 3) lds_add(W_PAUSE, (((turn >> (8 * w)) >> 8) & 255) - ((turn >> (8 * w)) & 255));
      if (c & 3) { wait_vm<16>(); if ((c & 3) == 1) lds_wait_ge(W_AFULL, c - 1); lds_add(W_AFULL, 1); }
    }
    wait_vm<0>(); lds_add(W_AFULL, 1);
    served = true;
  };
  float floorv = -__builtin_inff(); unsigned force = 0;
  if (ROLE_B()) {
    NEWLANE();
    const unsigned s0 = src_lane<8>(lane), s16 = s0 ^ 16u, s32 = s0 ^ 32u;
    const unsigned ka = rd_rows(lane) + bring, va0 = rd_cols(lane) + bring + 8192, va1 = va0 ^ 8u;
    const unsigned pmask = 0xff00u | (17u << w);
    auto rest = [&]() INL {
      if (lds_wait_clear_or(W_PAUSE, pmask, 16u << w) & (16u << w)) { serve(s0, s16, s32); lds_wait_clear_or(W_PAUSE, pmask, 0); }
    };
    unsigned pz = 0;
#pragma unroll 1
    for (unsigned c = 0; c < 16; ++c) {
      const unsigned so = ((c + 1 + cb) & 15) * 8192, so2 = so + 4096; unsigned ps = 0;
      const bool more = c != 15, morek = more && !(late && c == 0);
      att_chunk(A, floorv, force, va0, va1, ka, [&](auto mc) {
        constexpr int m = decltype(mc)::value;
        if constexpr (m == 44 || m == 30) pz = lds_peek(W_PAUSE);
        if constexpr (m == 1 || m == 35) ps = to_sgpr(pz);
        if constexpr (m == 4 || m == 38) { if (m == 4 ? morek : more) {
          if (__builtin_expect(ps & pmask, 0)) rest();
          if (m == 4) dma4(bring, sk, s0, s0, s32, s32, so); else dma4(bring + 8192, sv, s0, s16, s0, s16, so); } }
        if constexpr (m == 5 || m == 40) { if (m == 5 ? morek : more) { if (m == 5) dma4(bring + 4096, sk, s0, s0, s32, s32, so2); else dma4(bring + 12288, sv, s0, s16, s0, s16, so2); } }
        if constexpr (m == 16) { wait_vm<8>(); if (!more) wait_vm<0>(); }
        if constexpr (m == 43) wait_vm<8>();
      });
      floorv = 0.f; force = 1;
    }
  }
  if (!ROLE_B()) {
    floorv = -__builtin_inff(); force = 0;
    NEWLANE();
    const unsigned kl = rd_rows(lane) + RX, vl = rd_cols(lane) + RX + 8192;
    att_read_k(A, kl + (RK0 - RX));
#pragma unroll 1
    for (unsigned c = 0; c < 16; ++c) {
      const unsigned need = c < 15 ? c + 2 : 16;
      const unsigned va0 = vl + (c % NSA) * 16384, va1 = va0 ^ 8u, ka = kl + ((c + 1) % NSA) * 16384;
      unsigned bz = 0, bs = 0;
      att_chunk(A, floorv, force, va0, va1, ka, [&](auto mc) {
        constexpr int m = decltype(mc)::value;
        if constexpr (m == 15) { if (__builtin_expect(c == 0, 0)) lds_wait_ge(W_AFULL, 1); }
        if constexpr (m == 36) bz = lds_peek(W_AFULL);
        if constexpr (m == 39) lds_add(W_AFREE, 1u << (8 * w));
        if constexpr (m == 40) bs = to_sgpr(bz);
        if constexpr (m == 42) { if (__builtin_expect(bs < need, 0)) lds_wait_ge(W_AFULL, need); }
      });
      floorv = 0.f; force = 1;
    }
    wait_until(t0, (pace >> 13) & 8191);
    lds_add(W_PAUSE, 256u);
    lds_wait_all_ge(W_AFREE, 17);
  }
  { const bool rb = ROLE_B();
    wait_lgkm0();
    NEWLANE();
    att_store_o(A, (rb ? bring : RX + w * 12288) + lane * 16);
    lds_add(rb ? W_OB : W_ABAR, rb ? 4u << (8 * w) : 1u); }

  if (ROLE_B()) {
    NEWLANE();
    if (!served) { lds_wait_set(W_PAUSE, 16u << w); const unsigned s0 = src_lane<8>(lane); serve(s0, s0 ^ 16u, s0 ^ 32u); }
    const u64 tok = get_token();
    lds_wait_ge(W_WDONE, 4);
    reduce_out(p.part + (size_t)((w >> 1) * 256 + b * 8) * 49152, p.flags + b * 128, p.out + (size_t)b * 98304 + (w >> 1) * 1024, j, w, tok, lane);
    if (j == 0 && w == 0 && lane == 0) __hip_atomic_store((gu64*)epoch, tok, __ATOMIC_RELAXED, __HIP_MEMORY_SCOPE_AGENT);
  } else {
    NEWLANE();
    gemm_cfg<10>(G, p.wo[0] + (size_t)j * 524288 + w * 256, ring, RX + lane * 16, b & 15, W_XB, lane);
    w_step<10>(ring, G.srd, G, 0, b & 15);
    lds_wait_ge(W_ABAR, 8);
    const u32x4 swb = make_srd(p.wo[1] + (size_t)j * 524288 + w * 256, (511u << 10) + 256u);
    gemm<10, 2, 12288>(acc, G, [&](int i) INL {
      if (i >= 0 && i < 8) {
        const unsigned so = w_off<10>(0, i >> 2, b & 3), so1 = w_odd<10>(so);
        if ((i & 3) == 0) { set_m0(ring + (i >> 2) * 4096); w_piece<0>(swb, G, so, so1); } else if ((i & 3) == 1) w_piece<1>(swb, G, so, so1);
        else if ((i & 3) == 2) w_piece<2>(swb, G, so, so1); else w_piece<3>(swb, G, so, so1); }
      if (i == 4) lds_add(W_ABAR, 1);
      if (i == 11) { lds_wait_ge(W_ABAR, 12); w_step<10>(RX + w * 8192, swb, G, 1, b & 3); lds_add(W_PAUSE, 0u - 256u); }
    });
    NEWLANE();
    char* const part = p.part + (size_t)((b * 8 + j) * 4 + w) * 12288;
    const unsigned sz0 = j == 2 * w ? 0u : 12288u, sz1 = j == 2 * w + 1 ? 0u : 12288u, pl = lane * 16;
    const __amdgpu_buffer_rsrc_t ra0 = __builtin_amdgcn_make_buffer_rsrc(part, 0, sz0, 0x00020000), ra1 = __builtin_amdgcn_make_buffer_rsrc(part, 0, sz1, 0x00020000);
    const __amdgpu_buffer_rsrc_t rb0 = __builtin_amdgcn_make_buffer_rsrc(part + (size_t)256 * 49152, 0, sz0, 0x00020000);
    const __amdgpu_buffer_rsrc_t rb1 = __builtin_amdgcn_make_buffer_rsrc(part + (size_t)256 * 49152, 0, sz1, 0x00020000);
    u32x4 pa[12];
#pragma unroll
    for (int f = 0; f < 12; ++f) { pa[f] = pack_raw(acc[2 * (f / 3)][f % 3], acc[2 * (f / 3) + 1][f % 3]); asm volatile("" : "+v"(pa[f])); }
#pragma unroll
    for (int e = 0; e < 2; ++e) if (j == 2 * w + e) {
#pragma unroll
      for (int f = 0; f < 6; ++f) *LDS_PTR(u32x4, ROWN + f * 1024 + pl) = pa[6 * e + f]; }
    auto put_a = [&](int f) INL { __builtin_amdgcn_raw_buffer_store_b128(pa[f], f < 6 ? ra0 : ra1, pl + f * 1024, 0, 16); };
    auto put_b = [&](int a) INL {
#pragma unroll
      for (int q = 0; q < 3; ++q) __builtin_amdgcn_raw_buffer_store_b128(pack_raw(acc[2 * a][q], acc[2 * a + 1][q]), a < 2 ? rb0 : rb1, pl + (a * 3 + q) * 1024, 0, 16);
      if (j == 2 * w + (a >> 1)) {
#pragma unroll
        for (int q = 0; q < 3; ++q) *LDS_PTR(u32x4, ROWN + 6144 + ((a & 1) * 3 + q) * 1024 + pl) = pack_raw(acc[2 * a][q], acc[2 * a + 1][q]); } };
    gemm_cfg<10>(G, p.wo[1] + (size_t)j * 524288 + w * 256, RX + w * 8192, RB + pl, b & 3, W_OB, lane);
    gemm<10, 4, 16384>(acc, G, [&](int i) INL {
      if (i == -1) lds_add(W_WDONE, 1);
      if (i >= 0 && i < 8) put_a(i);
      if (i == 8 || i == 9) { put_a(2 * i - 8); put_a(2 * i - 7); }
      if (i == 11 || i == 13 || i == 15) put_b((i - 11) / 2);
    });
    put_b(3);
    wait_lgkm0(); lds_add(W_SENT, 1);
    publish(p.flags + b * 128, j, w, get_token(), lane);
  }
}

extern "C" void kernel_launch(void* const* d_in, const int* in_sizes, int n_in, void* d_out, int out_size, void* d_ws, size_t ws_size, hipStream_t stream) {
  Params p; Pace pc;
  for (int br = 0; br < 2; ++br) {
    p.x[br] = (const char*)d_in[br * 5 + 0]; p.k[br] = (const char*)d_in[br * 5 + 1]; p.v[br] = (const char*)d_in[br * 5 + 2];
    p.wq[br] = (const char*)d_in[br * 5 + 3]; p.wo[br] = (const char*)d_in[br * 5 + 4];
  }
  p.part = (char*)d_ws; p.flags = (u64*)((char*)d_ws + (32u << 20)); p.out = (char*)d_out;
  const unsigned tq[4] = {1004, 2521, 1076, 1714}, dq[4] = {31, 76, 109, 120}, to[4] = {3104, 0, 3755, 4179}, dd[4] = {68, 0, 43, 68}, nb[4] = {0, 1, 1, 2};
  for (unsigned b = 0; b < 32; ++b) { const unsigned c = 2 * (b & 1) + (b >> 4), r = (b >> 1) & 7; pc.t[b] = (tq[c] + dq[c] * r) | (to[c] + dd[c] * r) << 13 | nb[c] << 26; }
  fused_k<<<256, 512, 0, stream>>>(p, pc);
}
```
